```python
import torch

def _softmax(x, axis=-1):
    return torch.softmax(x, dim=axis)
def _gelu(x):
    return torch.nn.functional.gelu(x, approximate="tanh")
def _silu(x):
    return torch.nn.functional.silu(x)
def _sigmoid(x):
    return torch.sigmoid(x)
def _layernorm(x, axis=-1, eps=1e-5):
    m = x.mean(dim=axis, keepdim=True)
    v = x.var(dim=axis, keepdim=True, unbiased=False)
    return (x - m) / torch.sqrt(v + eps)
def _scatter_add(tgt, idx, src, axis=0):
    return tgt.clone().index_add_(axis, idx, src)
def _segment_sum(src, seg, n):
    out = torch.zeros((n,) + src.shape[1:], dtype=src.dtype)
    return out.index_add_(0, seg, src)
def _pad(x, pw):
    flat = tuple(v for pair in reversed(pw) for v in pair)
    return torch.nn.functional.pad(x, flat)

kernel_name = 'standalone__spectral_conv_chebnet__graph'

def setup_inputs():

    _w = (lambda a: (lambda t: t.to(torch.bfloat16) if t.is_floating_point() else t)(torch.as_tensor(a)))
    N, deg, D, K = 50000, 16, 64, 3
    E = N * deg
    src = (lambda lo, hi, *s: torch.randint(lo, hi, s))(0, N, E).to(torch.int64)
    dst = (lambda lo, hi, *s: torch.randint(lo, hi, s))(0, N, E).to(torch.int64)
    deg_cnt = torch.bincount(dst, minlength=N).to(float) + 1.0
    dis = (1.0 / (lambda _x: torch.sqrt(torch.as_tensor(_x)))(deg_cnt)).to(float)
    ew = -(dis[src] * dis[dst])
    return {
        "x": _w((lambda *s: torch.randn(*s, dtype=torch.bfloat16) * 0.05)(N, D)),
        "src": _w(src),
        "dst": _w(dst),
        "ew": _w(ew),
        "w": _w((lambda *s: torch.randn(*s, dtype=torch.bfloat16) * 0.05)(K, D, D) * 0.2),
    }

def reference(x, src, dst, ew, w):
    N, K = x.shape[0], w.shape[0]
    Ts = [x]
    if K > 1:
        Ts.append(_segment_sum(ew[:, None] * x[src], dst, N))
    for k in range(2, K):
        Tk = 2.0 * _segment_sum(ew[:, None] * Ts[-1][src], dst, N) - Ts[-2]
        Ts.append(Tk)
    T = torch.stack(Ts, 0)
    return torch.einsum('knd,kde->ne', T, w)
```

```cpp
#include <hip/hip_runtime.h>
#include <hip/hip_bf16.h>
#include <stdint.h>

typedef unsigned int u32;
typedef unsigned short u16;
typedef unsigned char u8;
typedef u32 u32x4 __attribute__((ext_vector_type(4)));
typedef u32 u32x2 __attribute__((ext_vector_type(2)));
typedef int i32x4 __attribute__((ext_vector_type(4)));
typedef short s16x2 __attribute__((ext_vector_type(2)));
typedef short s16x4 __attribute__((ext_vector_type(4)));
typedef __bf16 bf16x8 __attribute__((ext_vector_type(8)));
typedef __bf16 bf16x2 __attribute__((ext_vector_type(2)));
typedef float f32x4 __attribute__((ext_vector_type(4)));
#define LAS __attribute__((address_space(3)))

constexpr int NN = 50000, NE = 800000;
constexpr int NG = 3125;
constexpr int NBK = 256;
constexpr int NB1 = 250, CH = 3200;
constexpr int GPB = 13;
constexpr int SLOTS = 24;
constexpr int CAP2 = 192;
constexpr int STEPB = 192;
constexpr int GLB = 12 * STEPB;

#define PF1 12288
#define PF2A 8192
#define PF2B 1
#define PF2C 1
#define PF3 4096

constexpr int L_RING = 0;
constexpr int L_LIST = L_RING + GPB * 8192;
constexpr int L_CNT = L_LIST + GPB * GLB;
constexpr int L_JUNK = L_CNT + 256;
constexpr int L_W = L_JUNK + 256;
constexpr int L_K2 = L_W;
constexpr int L_K3 = L_W + 24576;
static_assert(L_K3 <= 163840, "lds");
static_assert(L_W % 16 == 0 && L_LIST % 16 == 0 && L_CNT % 16 == 0, "align");

constexpr size_t W_LINE = 0;
constexpr size_t W_OVF = W_LINE + (size_t)NBK * NB1 * 128;
constexpr size_t W_OVFN = W_OVF + (size_t)NB1 * CH * 8;
constexpr size_t W_LIST = W_OVFN + 1024;
constexpr size_t W_GMSK = W_LIST + (size_t)NG * GLB + 4096;
constexpr size_t W_T1 = W_GMSK + 16384;
static_assert(W_OVF % 128 == 0 && W_LIST % 128 == 0 && W_GMSK % 128 == 0 && W_T1 % 128 == 0, "align");

__device__ __forceinline__ u32 lds_addr(const void* p) { return (u32)(uintptr_t)(const LAS char*)p; }
__device__ __forceinline__ int gstart(int b) { return (b * NG) >> 8; }
__device__ __forceinline__ u32 bucket_of_group(u32 g) { return ((g + 1u) * 256u - 1u) / (u32)NG; }

__device__ __forceinline__ u32x4 make_rsrc(const void* base, u32 bytes) {
    const uint64_t a = (uint64_t)base;
    u32x4 r;
    r[0] = (u32)a;
    r[1] = (u32)(a >> 32) & 0xffffu;
    r[2] = bytes;
    r[3] = 0x00020000u;
    return r;
}

__device__ __forceinline__ void dma4(u32x4 rsrc, u32 lds, u32 o0, u32 o1, u32 o2, u32 o3) {
    u32 keep;
    asm volatile(
        "s_waitcnt lgkmcnt(0)\n\t"
        "s_mov_b32 %0, m0\n\t"
        "s_mov_b32 m0, %1\n\t"
        "s_nop 0\n\t"
        "buffer_load_dwordx4 %2, %6, 0 offen lds\n\t"
        "buffer_load_dwordx4 %3, %6, 0 offen offset:1024 lds\n\t"
        "buffer_load_dwordx4 %4, %6, 0 offen offset:2048 lds\n\t"
        "buffer_load_dwordx4 %5, %6, 0 offen offset:3072 lds\n\t"
        "s_mov_b32 m0, %0"
        : "=&s"(keep)
        : "s"(lds), "v"(o0), "v"(o1), "v"(o2), "v"(o3), "s"(rsrc)
        : "memory");
}
__device__ __forceinline__ void dma1(u32x4 rsrc, u32 lds, u32 o0) {
    u32 keep;
    asm volatile(
        "s_mov_b32 %0, m0\n\t"
        "s_mov_b32 m0, %1\n\t"
        "s_nop 0\n\t"
        "buffer_load_dwordx4 %2, %3, 0 offen lds\n\t"
        "s_mov_b32 m0, %0"
        : "=&s"(keep)
        : "s"(lds), "v"(o0), "s"(rsrc)
        : "memory");
}
__device__ __forceinline__ void touch(u32x4 rsrc, u32 junk_lds, u32 off) {
    u32 keep;
    asm volatile(
        "s_mov_b32 %0, m0\n\t"
        "s_mov_b32 m0, %1\n\t"
        "s_nop 0\n\t"
        "buffer_load_dword %2, %3, 0 offen lds\n\t"
        "s_mov_b32 m0, %0"
        : "=&s"(keep)
        : "s"(junk_lds), "v"(off), "s"(rsrc)
        : "memory");
}
#define WAIT_VM(n) asm volatile("s_waitcnt vmcnt(" #n ")" ::: "memory")
__device__ __forceinline__ void st16(void* p, u32x4 v) {
    asm volatile("global_store_dwordx4 %0, %1, off sc1\n\ts_nop 1" ::"v"(p), "v"(v) : "memory");
}
__device__ __forceinline__ void st8(void* p, u32x2 v) {
    asm volatile("global_store_dwordx2 %0, %1, off sc1" ::"v"(p), "v"(v) : "memory");
}

#define STAMP(k, i) do {} while (0)

__device__ __forceinline__ u32 pack_bf16(float a, float b) {
    bf16x2 v;
    v[0] = (__bf16)a;
    v[1] = (__bf16)b;
    return __builtin_bit_cast(u32, v);
}
__device__ __forceinline__ float bf_lo(u32 v) { return __uint_as_float(v << 16); }
__device__ __forceinline__ float bf_hi(u32 v) { return __uint_as_float(v & 0xffff0000u); }

__global__ __launch_bounds__(1024) void k1_partition(const i32x4* __restrict__ src4, const i32x4* __restrict__ dst4,
                                                     const u32x2* __restrict__ ew4, char* __restrict__ lines,
                                                     u32x2* __restrict__ ovf, u32* __restrict__ ovfn,
                                                     const char* __restrict__ x) {
    __shared__ __attribute__((aligned(16))) u32 stage[4 * 2048];
    __shared__ __attribute__((aligned(16))) u32 cnt[256 + 64 + 4];
    const int t = threadIdx.x, blk = blockIdx.x;
    STAMP(0, 0);
    i32x4 d = 0, s = 0;
    u32x2 w = 0;
    if (t < CH / 4) {
        d = __builtin_nontemporal_load(dst4 + blk * (CH / 4) + t);
        s = __builtin_nontemporal_load(src4 + blk * (CH / 4) + t);
        w = __builtin_nontemporal_load(ew4 + blk * (CH / 4) + t);
    }
    {
        const u32x4 ones = 0xffffffffu;
        if (t < 512) *(u32x4*)&stage[3 * 2048 + t * 4] = ones;
        else if (t < 512 + 81) *(u32x4*)&cnt[(t - 512) * 4] = (u32x4)0u;
    }
    __syncthreads();
    if (t < CH / 4) {
        u32 rk[4], bk[4], mt[4];
#pragma unroll
        for (int i = 0; i < 4; ++i) {
            const u32 dd = (u32)d[i];
            const u32 g = dd >> 4;
            const u32 b = bucket_of_group(g);
            bk[i] = b;
            mt[i] = ((g - ((b * (u32)NG) >> 8)) << 4) | (dd & 15u);
            rk[i] = atomicAdd(&cnt[b], 1u);
        }
#pragma unroll
        for (int i = 0; i < 4; ++i) {
            const u32 lo = (u32)s[i] | ((i & 1) ? (w[i >> 1] & 0xffff0000u) : (w[i >> 1] << 16));
            const u32 c = bk[i] * 8u + (rk[i] & 7u), sub = rk[i] >> 3;
            if (rk[i] < (u32)SLOTS) {
                stage[sub * 2048u + c] = lo;
                ((u8*)&stage[3 * 2048 + c])[sub] = (u8)mt[i];
            } else {
                const u32 o = atomicAdd(&cnt[320], 1u);
                u32x2 r;
                r[0] = lo; r[1] = (u32)d[i];
                ovf[blk * CH + o] = r;
                ((u8*)&stage[3 * 2048 + bk[i] * 8u])[3] = 0;
            }
        }
    }
    STAMP(0, 1);
    __syncthreads();
    STAMP(0, 2);
#pragma unroll
    for (int h = 0; h < 2; ++h) {
        const int c = t + h * 1024;
        u32x4 v;
        v[0] = stage[c]; v[1] = stage[2048 + c]; v[2] = stage[4096 + c]; v[3] = stage[6144 + c];
        st16(lines + ((size_t)(c >> 3) * NB1 + blk) * 128 + (c & 7) * 16, v);
    }
    if (t == 0) ovfn[blk] = cnt[320];
    if (PF1 > 0) {
        const int r = blk >> 3, nr = (NB1 + 7 - (blk & 7)) >> 3;
        const u32x4 xrs = make_rsrc(x, (u32)PF1 * 128u);
        const int i = t * nr + r;
        if (__builtin_amdgcn_readfirstlane(t) * nr < PF1) touch(xrs, lds_addr(&cnt[256]), (u32)i * 128u);
    }
    STAMP(0, 4);
}

struct Lane {
    u32 coff[4];
    u32 tra[4];
    u32 sh;
    int lane;
};
__device__ __forceinline__ Lane make_lane(int lane) {
    Lane L;
    L.lane = lane;
    const int a = lane >> 3, p = lane & 7;
#pragma unroll
    for (int i = 0; i < 4; ++i) L.coff[i] = (u32)(((p ^ ((((a >> 1) & 1) << 1) | ((i & 1) << 2))) << 4) + 4096 - 1024 * i);
    const int g = lane >> 4, q = (lane & 15) >> 2, pp = lane & 3;
    const int sw = ((g & 1) << 1) | ((q & 1) << 2);
#pragma unroll
    for (int c = 0; c < 4; ++c)
        L.tra[c] = (u32)(128 * (8 * (q & 1) + 2 * g + (q >> 1)) + 16 * ((2 * c + (pp >> 1)) ^ sw) + 8 * (pp & 1));
    L.sh = (u32)(15 - (lane & 15)) * 0x10001u;
    return L;
}
__device__ __forceinline__ bf16x8 afrag(const char* buf, const Lane& L, int c) {
    const s16x4 a0 = __builtin_amdgcn_ds_read_tr16_b64_v4i16((LAS s16x4*)(buf + L.tra[c]));
    const s16x4 a1 = __builtin_amdgcn_ds_read_tr16_b64_v4i16((LAS s16x4*)(buf + L.tra[c] + 2048));
    return __builtin_bit_cast(bf16x8, __builtin_shufflevector(a0, a1, 0, 1, 2, 3, 4, 5, 6, 7));
}
__device__ __forceinline__ u32 row_off(u32 lo, u32 add) {
    u32 r;
    asm("v_mad_u32_u16 %0, %1, %2, %3" : "=v"(r) : "v"(lo), "s"(128u), "v"(add));
    return r;
}
__device__ __forceinline__ void issue_step(u32x4 rsrc, const char* list, int s, char* buf, const Lane& L) {
    const u32x4 ix = *(const u32x4*)(list + s * STEPB + (L.lane >> 3) * 16);
    dma4(rsrc, lds_addr(buf), row_off(ix[0], L.coff[0]), row_off(ix[1], L.coff[1]), row_off(ix[2], L.coff[2]), row_off(ix[3], L.coff[3]));
}
__device__ __forceinline__ u32 sel2(u32 oh, u32 lo0, u32 lo1, u32 sh) {
    const s16x2 m = (__builtin_bit_cast(s16x2, oh) << __builtin_bit_cast(s16x2, sh)) >> 15;
    return __builtin_amdgcn_perm(lo1, lo0, 0x07060302u) & __builtin_bit_cast(u32, m);
}
__device__ __forceinline__ int pop_step(u32& m) { const int s = __builtin_ctz(m); m &= m - 1u; return s; }
template <bool PRIMED>
__device__ __forceinline__ void gather_group(f32x4 (&acc)[4], u32x4 rsrc, const char* list, char* ring, u32 mask, const Lane& L) {
    u32 im = mask;
    if (im) { const int s = pop_step(im); if (!PRIMED) issue_step(rsrc, list, s, ring, L); }
    if (im) { const int s = pop_step(im); if (!PRIMED) issue_step(rsrc, list, s, ring + 4096, L); }
    int par = 0;
    while (mask) {
        const int s = pop_step(mask);
        char* buf = ring + par;
        par ^= 4096;
        const char* st = list + s * STEPB;
        const u32x4 l0 = *(const u32x4*)(st + (L.lane >> 4) * 32);
        const u32x4 l1 = *(const u32x4*)(st + (L.lane >> 4) * 32 + 16);
        const u32x4 oh = *(const u32x4*)(st + 128 + (L.lane >> 4) * 16);
        if (mask) WAIT_VM(4); else WAIT_VM(0);
        bf16x8 a[4];
#pragma unroll
        for (int c = 0; c < 4; ++c) a[c] = afrag(buf, L, c);
        if (im) issue_step(rsrc, list, pop_step(im), buf, L);
        u32x4 b;
        b[0] = sel2(oh[0], l0[0], l0[1], L.sh);
        b[1] = sel2(oh[2], l1[0], l1[1], L.sh);
        b[2] = sel2(oh[1], l0[2], l0[3], L.sh);
        b[3] = sel2(oh[3], l1[2], l1[3], L.sh);
#pragma unroll
        for (int c = 0; c < 4; ++c)
            acc[c] = __builtin_amdgcn_mfma_f32_16x16x32_bf16(a[c], __builtin_bit_cast(bf16x8, b), acc[c], 0, 0, 0);
    }
}

__device__ __forceinline__ void put_record(char* smem, u32 lo, u32 meta) {
    const u32 sr = ((lo & 0xffffu) * 1342u) >> 24;
    const u32 g = meta >> 4;
    const u32 rank = atomicAdd((u32*)(smem + L_CNT) + g * 4u + sr, 1u);
    if (rank < (u32)CAP2) {
        const u32 slot = (sr >> 1) * CAP2 + ((sr & 1u) ? (u32)(CAP2 - 1) - rank : rank);
        char* base = smem + L_LIST + g * GLB + (slot >> 5) * STEPB;
        *(u32*)(base + (slot & 31u) * 4u) = lo;
        *(u16*)(base + 128 + (slot & 31u) * 2u) = (u16)(1u << (meta & 15u));
    }
}
__device__ __forceinline__ void put_chunk(char* smem, u32x4 v) {
#pragma unroll
    for (int j = 0; j < 3; ++j) {
        const u32 meta = (v[3] >> (8 * j)) & 0xffu;
        if (meta != 0xffu) put_record(smem, v[j], meta);
    }
}
__device__ __forceinline__ void put_surplus(char* smem, u32x2 r, u32 bucket) {
    const u32 g = r[1] >> 4;
    if (bucket_of_group(g) == bucket) put_record(smem, r[0], ((g - (u32)gstart(bucket)) << 4) | (r[1] & 15u));
}
__device__ __forceinline__ void init_lists(char* smem, int t) {
    constexpr int NV = GPB * GLB / 16;
#pragma unroll
    for (int h = 0; h < 2; ++h) {
        const int i = t + h * 1024;
        if (i < NV) *(u32x4*)(smem + L_LIST + i * 16) = (u32x4)((i % 12) < 8 ? 0xffffffffu : 0u);
        else if (i < NV + 16) *(u32x4*)(smem + L_LIST + i * 16) = (u32x4)0u;
    }
}
__device__ __forceinline__ u32 step_mask(const char* smem, int wv) {
    const u32x4 c = *(const u32x4*)(smem + L_CNT + wv * 16);
    u32 m = 0;
#pragma unroll
    for (int h = 0; h < 2; ++h) {
        const u32 lo = (c[2 * h] + 31u) >> 5, hi = (c[2 * h + 1] + 31u) >> 5;
        m |= (((1u << lo) - 1u) | ((0x3fu << (6u - hi)) & 0x3fu)) << (6 * h);
    }
    return __builtin_amdgcn_readfirstlane(m);
}

__device__ __forceinline__ void slow_bucket(char* smem, f32x4 (&acc)[4], const u32x4* __restrict__ bl,
                                            const u32x2* __restrict__ ovf, const u32* __restrict__ ovfn, u32 bucket,
                                            u32x4 rsrc, int t, int wv, const Lane& L) {
    int blk = -((NB1 + 7) / 8);
    u32 o0 = 0, n = 0;
    for (;;) {
        if (blk >= 0 && o0 >= n) {
            for (; blk < NB1; ++blk) if ((bl[blk * 8][3] >> 24) == 0u) break;
            if (blk >= NB1) break;
            n = ovfn[blk];
            o0 = 0;
        }
        __syncthreads();
        init_lists(smem, t);
        __syncthreads();
        if (blk < 0) {
            const int c = (blk + (NB1 + 7) / 8) * 64 + t;
            if (t < 64 && c < NB1 * 8) put_chunk(smem, bl[c]);
            ++blk;
        } else {
            if (t < CAP2 && o0 + t < n) put_surplus(smem, ovf[blk * CH + o0 + t], bucket);
            o0 += CAP2;
            if (o0 >= n) ++blk;
        }
        __syncthreads();
        if (wv < GPB) gather_group<false>(acc, rsrc, smem + L_LIST + wv * GLB, smem + L_RING + wv * 8192, step_mask(smem, wv), L);
    }
}

__device__ __forceinline__ void store_tile(char* __restrict__ outp, int node0, const f32x4 (&acc)[4], int lane) {
    char* p = outp + (size_t)(node0 + (lane & 15)) * 128 + (lane >> 4) * 8;
#pragma unroll
    for (int c = 0; c < 4; ++c) {
        u32x2 v;
        v[0] = pack_bf16(acc[c][0], acc[c][1]);
        v[1] = pack_bf16(acc[c][2], acc[c][3]);
        *(u32x2*)(p + c * 32) = v;
    }
}

__global__ __launch_bounds__(1024) void k2_sort_spmm(const u32x4* __restrict__ lines, const u32x2* __restrict__ ovf,
                                                     const u32* __restrict__ ovfn, const char* __restrict__ x,
                                                     char* __restrict__ lists, u32* __restrict__ gmask,
                                                     char* __restrict__ t1, const char* __restrict__ w) {
    __shared__ __attribute__((aligned(16))) char smem[L_K2];
    const int t = threadIdx.x, lane = t & 63, wv = __builtin_amdgcn_readfirstlane(t >> 6);
    const int xr = blockIdx.x >> 3;
    const int bucket = (blockIdx.x & 7) * 32 + xr;
    const int g0 = gstart(bucket), ng = gstart(bucket + 1) - g0;
    STAMP(1, 0);
    const u32x4* bl = lines + (size_t)bucket * (NB1 * 8);
    u32x4 v0 = __builtin_nontemporal_load(bl + t), v1 = 0xffffffffu;
    if (t + 1024 < NB1 * 8) v1 = __builtin_nontemporal_load(bl + t + 1024);
    init_lists(smem, t);
    const u32x4 xrs = make_rsrc(x, (u32)NN * 128u);
    __syncthreads();
    asm volatile("" : "+v"(v0), "+v"(v1));
    if (PF2A > 0 && wv >= 12) {
        for (int i = (wv - 12) * 64 + lane; i < PF2A / 32; i += 256) touch(xrs, lds_addr(smem + L_JUNK), (u32)(PF1 + i * 32 + xr) * 128u);
    }
    put_chunk(smem, v0);
    put_chunk(smem, v1);
    {
        unsigned long long f0 = __ballot((t & 7) == 0 && (v0[3] >> 24) == 0u), f1 = __ballot((t & 7) == 0 && (v1[3] >> 24) == 0u);
        while (f0 | f1) {
            int c;
            if (f0) { c = __builtin_ctzll(f0); f0 &= f0 - 1; } else { c = __builtin_ctzll(f1) + 1024; f1 &= f1 - 1; }
            const int blk = (wv * 64 + c) >> 3;
            const u32x2 r0 = ovf[blk * CH + lane];
            const u32 n = ovfn[blk];
            if ((u32)lane < n) put_surplus(smem, r0, bucket);
            for (u32 o = 64 + lane; o < n; o += 64) put_surplus(smem, ovf[blk * CH + o], bucket);
        }
    }
    STAMP(1, 1);
    __syncthreads();
    STAMP(1, 2);
    const Lane L = make_lane(lane);
    const u32x4 rsrc = make_rsrc(x - 4096, (u32)NN * 128u + 4096u);
    f32x4 acc[4];
#pragma unroll
    for (int c = 0; c < 4; ++c) acc[c] = 0.f;
    bool slow;
    {
        const u32x2 c = *(const u32x2*)(smem + L_CNT + (lane & 31) * 8);
        slow = __any(lane < 2 * GPB && c[0] + c[1] > (u32)CAP2);
    }
    if (!slow) {
        if (wv >= GPB) {
            volatile LAS u32* flag = (volatile LAS u32*)(smem + L_CNT) + 61;
            if (wv == GPB) {
                if (PF2B) {
                    for (int i = lane; PF1 + PF2A + i * 32 < NN; i += 64)
                        touch(xrs, lds_addr(smem + L_JUNK), (u32)(PF1 + PF2A + i * 32 + xr) * 128u);
                    WAIT_VM(0);
                }
                if (PF2C) {
                    *flag = 1u;
                    for (int i = lane; i < ng * 16; i += 64) touch(xrs, lds_addr(smem + L_JUNK), (u32)(g0 * 16 + i) * 128u);
                    touch(make_rsrc(w, 192u * 128u), lds_addr(smem + L_JUNK), (u32)(lane * 32 + xr) * 128u);
                }
            } else {
                char* gl = lists + (size_t)g0 * GLB;
                const int n16 = ng * (GLB / 16), h = wv - GPB - 1;
                for (int i = h * 64 + lane; i < n16; i += 128) st16(gl + i * 16, *(const u32x4*)(smem + L_LIST + i * 16));
                if (PF2C) {
                    WAIT_VM(0);
                    for (int spin = 0; spin < 4096 && *flag == 0u; ++spin) __builtin_amdgcn_s_sleep(4);
                    const u32x4 lrs = make_rsrc(gl, (u32)(ng * GLB));
                    for (int i = lane; i < ng * 18; i += 64)
                        if (((i >> 3) & 1) == h) touch(lrs, lds_addr(smem + L_JUNK), (u32)i * 128u);
                }
            }
            STAMP(1, 6);
            return;
        }
        if (wv >= ng) return;
        const u32 mask = step_mask(smem, wv);
        gather_group<false>(acc, rsrc, smem + L_LIST + wv * GLB, smem + L_RING + wv * 8192, mask, L);
        if (lane == 0) gmask[g0 + wv] = mask;
    } else {
        slow_bucket(smem, acc, bl, ovf, ovfn, bucket, rsrc, t, wv, L);
        if (wv >= ng) return;
        if (lane == 0) gmask[g0 + wv] = 0xffffffffu;
    }
    STAMP(1, 4);
    store_tile(t1, (g0 + wv) * 16, acc, lane);
}

__global__ __launch_bounds__(1024) void k3_spmm_gemm(const u32x4* __restrict__ lines, const u32x2* __restrict__ ovf,
                                                     const u32* __restrict__ ovfn, const char* __restrict__ x,
                                                     const char* __restrict__ lists, const u32* __restrict__ gmask,
                                                     const char* __restrict__ t1, const char* __restrict__ w,
                                                     char* __restrict__ out) {
    __shared__ __attribute__((aligned(16))) char smem[L_K3];
    const int t = threadIdx.x, lane = t & 63, wv = __builtin_amdgcn_readfirstlane(t >> 6);
    const int xr = blockIdx.x >> 3;
    const int bucket = (blockIdx.x & 7) * 32 + xr;
    const int g0 = gstart(bucket), ng = gstart(bucket + 1) - g0;
    STAMP(2, 0);
    const bool active = wv < ng;
    const int g = active ? g0 + wv : g0;
    const Lane L = make_lane(lane);
    char* list = smem + L_LIST + wv * GLB;
    char* ring = smem + L_RING + wv * 8192;
    const u32 mk = gmask[g];
    u32x2 xf[4], tf[4];
    if (wv < GPB) {
        const u32x4 lrs = make_rsrc(lists, (u32)(NG * GLB));
        dma1(lrs, lds_addr(list), (u32)(g * GLB + lane * 16));
        dma1(lrs, lds_addr(list + 1024), (u32)(g * GLB + 1024 + lane * 16));
        if (lane < 16) dma1(lrs, lds_addr(list + 2048), (u32)(g * GLB + 2048 + lane * 16));
    } else {
        const u32x4 wrs = make_rsrc(w - 4096, 3u * 64u * 128u + 4096u);
        const int a = lane >> 3;
#pragma unroll
        for (int h = 0; h < 2; ++h) {
            const int ks = (wv - GPB) * 2 + h;
            const u32 rb = (u32)(32 * ks + 4 * (a >> 1) + 2 * (a & 1)) * 128u;
            dma4(wrs, lds_addr(smem + L_W + ks * 4096), rb + L.coff[0], rb + 128u + L.coff[1], rb + 2048u + L.coff[2], rb + 2176u + L.coff[3]);
        }
        if (PF3 > 0) {
            const u32x4 trs = make_rsrc(t1, (u32)PF3 * 128u);
            touch(trs, lds_addr(smem + L_JUNK), (u32)(((wv - GPB) * 64 + lane) * 32 + xr) * 128u);
        }
    }
    const u32 mku = __builtin_amdgcn_readfirstlane(mk);
    const bool slow = mku == 0xffffffffu;
    const u32 mask = (slow || !active) ? 0u : mku;
    if (PF3 > 0 && wv >= GPB) WAIT_VM(1); else WAIT_VM(0);
    STAMP(2, 1);
    __builtin_amdgcn_s_barrier();
    asm volatile("" ::: "memory");
    const u32x4 rsrc = make_rsrc(t1 - 4096, (u32)NN * 128u + 4096u);
    if (active) {
        const size_t o = (size_t)(g * 16 + (lane & 15)) * 128 + (lane >> 4) * 8;
#pragma unroll
        for (int c = 0; c < 4; ++c) { xf[c] = *(const u32x2*)(x + o + c * 32); tf[c] = *(const u32x2*)(t1 + o + c * 32); }
    }
    {
        u32 im = mask;
        if (im) issue_step(rsrc, list, pop_step(im), ring, L);
        if (im) issue_step(rsrc, list, pop_step(im), ring + 4096, L);
    }
    f32x4 acc[4];
#pragma unroll
    for (int c = 0; c < 4; ++c) acc[c] = 0.f;
    STAMP(2, 2);
    if (!slow) {
        if (!active) return;
        gather_group<true>(acc, rsrc, list, ring, mask, L);
    } else {
        slow_bucket(smem, acc, lines + (size_t)bucket * (NB1 * 8), ovf, ovfn, bucket, rsrc, t, wv, L);
        if (!active) return;
    }
    STAMP(2, 3);
    f32x4 o[4];
#pragma unroll
    for (int c = 0; c < 4; ++c) o[c] = 0.f;
#pragma unroll
    for (int kk = 0; kk < 2; ++kk) {
        u32x4 bx, bt, b2;
        bx[0] = xf[2 * kk][0]; bx[1] = xf[2 * kk][1]; bx[2] = xf[2 * kk + 1][0]; bx[3] = xf[2 * kk + 1][1];
        bt[0] = tf[2 * kk][0]; bt[1] = tf[2 * kk][1]; bt[2] = tf[2 * kk + 1][0]; bt[3] = tf[2 * kk + 1][1];
#pragma unroll
        for (int h = 0; h < 2; ++h) {
            const int c = 2 * kk + h;
            b2[2 * h] = pack_bf16(2.f * acc[c][0] - bf_lo(xf[c][0]), 2.f * acc[c][1] - bf_hi(xf[c][0]));
            b2[2 * h + 1] = pack_bf16(2.f * acc[c][2] - bf_lo(xf[c][1]), 2.f * acc[c][3] - bf_hi(xf[c][1]));
        }
#pragma unroll
        for (int c = 0; c < 4; ++c) {
            o[c] = __builtin_amdgcn_mfma_f32_16x16x32_bf16(afrag(smem + L_W + kk * 4096, L, c), __builtin_bit_cast(bf16x8, bx), o[c], 0, 0, 0);
            o[c] = __builtin_amdgcn_mfma_f32_16x16x32_bf16(afrag(smem + L_W + (2 + kk) * 4096, L, c), __builtin_bit_cast(bf16x8, bt), o[c], 0, 0, 0);
            o[c] = __builtin_amdgcn_mfma_f32_16x16x32_bf16(afrag(smem + L_W + (4 + kk) * 4096, L, c), __builtin_bit_cast(bf16x8, b2), o[c], 0, 0, 0);
        }
    }
    store_tile(out, g * 16, o, lane);
}

extern "C" void kernel_launch(void* const* d_in, const int* in_sizes, int n_in, void* d_out, int out_size, void* d_ws,
                              size_t ws_size, hipStream_t stream) {
    const char* x = (const char*)d_in[0];
    const int* src = (const int*)d_in[1];
    const int* dst = (const int*)d_in[2];
    const void* ew = d_in[3];
    const char* w = (const char*)d_in[4];
    char* ws = (char*)d_ws;
    k1_partition<<<NB1, 1024, 0, stream>>>((const i32x4*)src, (const i32x4*)dst, (const u32x2*)ew, ws + W_LINE,
                                           (u32x2*)(ws + W_OVF), (u32*)(ws + W_OVFN), x);
    k2_sort_spmm<<<NBK, 1024, 0, stream>>>((const u32x4*)(ws + W_LINE), (const u32x2*)(ws + W_OVF), (const u32*)(ws + W_OVFN),
                                           x, ws + W_LIST, (u32*)(ws + W_GMSK), ws + W_T1, w);
    k3_spmm_gemm<<<NBK, 1024, 0, stream>>>((const u32x4*)(ws + W_LINE), (const u32x2*)(ws + W_OVF), (const u32*)(ws + W_OVFN),
                                           x, ws + W_LIST, (const u32*)(ws + W_GMSK), ws + W_T1, w, (char*)d_out);
}
```

```cpp
#include <hip/hip_runtime.h>
#include <hip/hip_bf16.h>
#include <stdint.h>

typedef unsigned int u32;
typedef unsigned short u16;
typedef unsigned char u8;
typedef u32 u32x4 __attribute__((ext_vector_type(4)));
typedef u32 u32x2 __attribute__((ext_vector_type(2)));
typedef int i32x4 __attribute__((ext_vector_type(4)));
typedef short s16x2 __attribute__((ext_vector_type(2)));
typedef short s16x4 __attribute__((ext_vector_type(4)));
typedef __bf16 bf16x8 __attribute__((ext_vector_type(8)));
typedef __bf16 bf16x2 __attribute__((ext_vector_type(2)));
typedef float f32x4 __attribute__((ext_vector_type(4)));
#define LAS __attribute__((address_space(3)))

constexpr int NN = 50000, NE = 800000;
constexpr int NG = 3125;
constexpr int NBK = 256;
constexpr int NB1 = 250, CH = 3200;
constexpr int GPB = 13;
constexpr int SLOTS = 24;
constexpr int CAP2 = 192;
constexpr int BLKB = 48;
constexpr int GLB = 48 * BLKB;

#define PF1 0
#define PF2A 0
#define PF2C 0
#define PF3 0
#define ORD2 0
#define PRIO 1

constexpr int L_RING = 0;
constexpr int L_LIST = L_RING + GPB * 8192;
constexpr int L_NUL = L_LIST + GPB * GLB;
constexpr int L_CNT = L_NUL + BLKB;
constexpr int L_JUNK = L_CNT + 256;
constexpr int L_W = L_JUNK + 256;
constexpr int L_K2 = L_W;
constexpr int L_K3 = L_W + 24576;
static_assert(L_K3 <= 163840, "lds");
static_assert(L_W % 16 == 0 && L_LIST % 16 == 0 && L_CNT % 16 == 0, "align");

constexpr size_t W_LINE = 0;
constexpr size_t W_OVF = W_LINE + (size_t)NBK * NB1 * 128;
constexpr size_t W_OVFN = W_OVF + (size_t)NB1 * CH * 8;
constexpr size_t W_LIST = W_OVFN + 1024;
constexpr size_t W_GMSK = W_LIST + (size_t)NG * GLB + 4096;
constexpr size_t W_T1 = W_GMSK + 16384;
static_assert(W_OVF % 128 == 0 && W_LIST % 128 == 0 && W_GMSK % 128 == 0 && W_T1 % 128 == 0, "align");

__device__ __forceinline__ u32 lds_addr(const void* p) { return (u32)(uintptr_t)(const LAS char*)p; }
__device__ __forceinline__ int gstart(int b) { return (b * NG) >> 8; }
__device__ __forceinline__ u32 bucket_of_group(u32 g) { return ((g + 1u) * 256u - 1u) / (u32)NG; }

__device__ __forceinline__ u32x4 make_rsrc(const void* base, u32 bytes) {
    const uint64_t a = (uint64_t)base;
    u32x4 r;
    r[0] = (u32)a;
    r[1] = (u32)(a >> 32) & 0xffffu;
    r[2] = bytes;
    r[3] = 0x00020000u;
    return r;
}

__device__ __forceinline__ void dma4(u32x4 rsrc, u32 lds, u32 o0, u32 o1, u32 o2, u32 o3) {
    u32 keep;
    asm volatile(
        "s_waitcnt lgkmcnt(0)\n\t"
        "s_mov_b32 %0, m0\n\t"
        "s_mov_b32 m0, %1\n\t"
        "s_nop 0\n\t"
        "buffer_load_dwordx4 %2, %6, 0 offen lds\n\t"
        "buffer_load_dwordx4 %3, %6, 0 offen offset:1024 lds\n\t"
        "buffer_load_dwordx4 %4, %6, 0 offen offset:2048 lds\n\t"
        "buffer_load_dwordx4 %5, %6, 0 offen offset:3072 lds\n\t"
        "s_mov_b32 m0, %0"
        : "=&s"(keep)
        : "s"(lds), "v"(o0), "v"(o1), "v"(o2), "v"(o3), "s"(rsrc)
        : "memory");
}
__device__ __forceinline__ void dma1(u32x4 rsrc, u32 lds, u32 o0) {
    u32 keep;
    asm volatile(
        "s_mov_b32 %0, m0\n\t"
        "s_mov_b32 m0, %1\n\t"
        "s_nop 0\n\t"
        "buffer_load_dwordx4 %2, %3, 0 offen lds\n\t"
        "s_mov_b32 m0, %0"
        : "=&s"(keep)
        : "s"(lds), "v"(o0), "s"(rsrc)
        : "memory");
}
__device__ __forceinline__ void touch(u32x4 rsrc, u32 junk_lds, u32 off) {
    u32 keep;
    asm volatile(
        "s_mov_b32 %0, m0\n\t"
        "s_mov_b32 m0, %1\n\t"
        "s_nop 0\n\t"
        "buffer_load_dword %2, %3, 0 offen lds\n\t"
        "s_mov_b32 m0, %0"
        : "=&s"(keep)
        : "s"(junk_lds), "v"(off), "s"(rsrc)
        : "memory");
}
#define WAIT_VM(n) asm volatile("s_waitcnt vmcnt(" #n ")" ::: "memory")
__device__ __forceinline__ void st16(void* p, u32x4 v) {
    asm volatile("global_store_dwordx4 %0, %1, off sc1\n\ts_nop 1" ::"v"(p), "v"(v) : "memory");
}
__device__ __forceinline__ void st8(void* p, u32x2 v) {
    asm volatile("global_store_dwordx2 %0, %1, off sc1" ::"v"(p), "v"(v) : "memory");
}

#define STAMP(k, i) do {} while (0)
#define CSTAMP(i) do {} while (0)

__device__ __forceinline__ u32 pack_bf16(float a, float b) {
    bf16x2 v;
    v[0] = (__bf16)a;
    v[1] = (__bf16)b;
    return __builtin_bit_cast(u32, v);
}
__device__ __forceinline__ float bf_lo(u32 v) { return __uint_as_float(v << 16); }
__device__ __forceinline__ float bf_hi(u32 v) { return __uint_as_float(v & 0xffff0000u); }

__global__ __launch_bounds__(1024) void k1_partition(const i32x4* __restrict__ src4, const i32x4* __restrict__ dst4,
                                                     const u32x2* __restrict__ ew4, char* __restrict__ lines,
                                                     u32x2* __restrict__ ovf, u32* __restrict__ ovfn,
                                                     const char* __restrict__ x) {
    __shared__ __attribute__((aligned(16))) u32 stage[4 * 2048];
    __shared__ __attribute__((aligned(16))) u32 cnt[256 + 64 + 4];
    const int t = threadIdx.x, blk = blockIdx.x;
    STAMP(0, 0);
    i32x4 d = 0, s = 0;
    u32x2 w = 0;
    if (t < CH / 4) {
        d = __builtin_nontemporal_load(dst4 + blk * (CH / 4) + t);
        s = __builtin_nontemporal_load(src4 + blk * (CH / 4) + t);
        w = __builtin_nontemporal_load(ew4 + blk * (CH / 4) + t);
    }
    {
        const u32x4 ones = 0xffffffffu;
        if (t < 512) *(u32x4*)&stage[3 * 2048 + t * 4] = ones;
        else if (t < 512 + 81) *(u32x4*)&cnt[(t - 512) * 4] = (u32x4)0u;
    }
    __syncthreads();
    if (t < CH / 4) {
        u32 rk[4], bk[4], mt[4];
#pragma unroll
        for (int i = 0; i < 4; ++i) {
            const u32 dd = (u32)d[i];
            const u32 g = dd >> 4;
            const u32 b = bucket_of_group(g);
            bk[i] = b;
            mt[i] = ((g - ((b * (u32)NG) >> 8)) << 4) | (dd & 15u);
            rk[i] = atomicAdd(&cnt[b], 1u);
        }
#pragma unroll
        for (int i = 0; i < 4; ++i) {
            const u32 lo = (u32)s[i] | ((i & 1) ? (w[i >> 1] & 0xffff0000u) : (w[i >> 1] << 16));
            const u32 c = bk[i] * 8u + (rk[i] & 7u), sub = rk[i] >> 3;
            if (rk[i] < (u32)SLOTS) {
                stage[sub * 2048u + c] = lo;
                ((u8*)&stage[3 * 2048 + c])[sub] = (u8)mt[i];
            } else {
                const u32 o = atomicAdd(&cnt[320], 1u);
                u32x2 r;
                r[0] = lo; r[1] = (u32)d[i];
                ovf[blk * CH + o] = r;
                ((u8*)&stage[3 * 2048 + bk[i] * 8u])[3] = 0;
            }
        }
    }
    STAMP(0, 1);
    __syncthreads();
    STAMP(0, 2);
#pragma unroll
    for (int h = 0; h < 2; ++h) {
        const int c = t + h * 1024;
        u32x4 v;
        v[0] = stage[c]; v[1] = stage[2048 + c]; v[2] = stage[4096 + c]; v[3] = stage[6144 + c];
        st16(lines + ((size_t)(c >> 3) * NB1 + blk) * 128 + (c & 7) * 16, v);
    }
    if (t == 0) ovfn[blk] = cnt[320];
    if (PF1 > 0) {
        const int r = blk >> 3, nr = (NB1 + 7 - (blk & 7)) >> 3;
        const u32x4 xrs = make_rsrc(x, (u32)PF1 * 128u);
        const int i0 = (__builtin_amdgcn_readfirstlane(t >> 6) * nr + r) * 64;
        if (i0 < PF1) touch(xrs, lds_addr(&cnt[256]), (u32)(i0 + (t & 63)) * 128u);
    }
    STAMP(0, 4);
}

struct Lane {
    u32 coff[4];
    u32 tra[4];
    u32 sh;
    int lane;
};
__device__ __forceinline__ Lane make_lane(int lane) {
    Lane L;
    L.lane = lane;
    const int a = lane >> 3, p = lane & 7;
#pragma unroll
    for (int i = 0; i < 4; ++i) L.coff[i] = (u32)(((p ^ ((((a >> 1) & 1) << 1) | ((i & 1) << 2))) << 4) + 4096 - 1024 * i);
    const int g = lane >> 4, q = (lane & 15) >> 2, pp = lane & 3;
    const int sw = ((g & 1) << 1) | ((q & 1) << 2);
#pragma unroll
    for (int c = 0; c < 4; ++c)
        L.tra[c] = (u32)(128 * (8 * (q & 1) + 2 * g + (q >> 1)) + 16 * ((2 * c + (pp >> 1)) ^ sw) + 8 * (pp & 1));
    L.sh = (u32)(15 - (lane & 15)) * 0x10001u;
    return L;
}
__device__ __forceinline__ bf16x8 afrag(const char* buf, const Lane& L, int c) {
    const s16x4 a0 = __builtin_amdgcn_ds_read_tr16_b64_v4i16((LAS s16x4*)(buf + L.tra[c]));
    const s16x4 a1 = __builtin_amdgcn_ds_read_tr16_b64_v4i16((LAS s16x4*)(buf + L.tra[c] + 2048));
    return __builtin_bit_cast(bf16x8, __builtin_shufflevector(a0, a1, 0, 1, 2, 3, 4, 5, 6, 7));
}
__device__ __forceinline__ u32 row_off(u32 lo, u32 add) {
    u32 r;
    asm("v_mad_u32_u16 %0, %1, %2, %3" : "=v"(r) : "v"(lo), "s"(128u), "v"(add));
    return r;
}
__device__ __forceinline__ u32 sel2(u32 oh, u32 lo0, u32 lo1, u32 sh) {
    const s16x2 m = (__builtin_bit_cast(s16x2, oh) << __builtin_bit_cast(s16x2, sh)) >> 15;
    return __builtin_amdgcn_perm(lo1, lo0, 0x07060302u) & __builtin_bit_cast(u32, m);
}
struct Plan {
    u32 e0, e1, e2, nb;
    u32 d0, d1, d2, d3;
    u32 nul;
    int ns;
};
__device__ __forceinline__ Plan make_plan(u32 word, u32 list, u32 nul, int rot) {
    const u32 n0 = word & 63u, s1 = (word >> 6) & 63u, e2 = (word >> 12) & 63u, s3 = (word >> 18) & 63u;
    u32 st[4] = {0u, s1, 24u, s3}, ln[4] = {n0, 24u - s1, e2 - 24u, 48u - s3};
    if (rot >= 1) { u32 a = st[0]; st[0] = st[1]; st[1] = a; a = ln[0]; ln[0] = ln[1]; ln[1] = a; }
    if (rot >= 2) { u32 a = st[1]; st[1] = st[2]; st[2] = a; a = ln[1]; ln[1] = ln[2]; ln[2] = a; }
    if (rot >= 3) { u32 a = st[2]; st[2] = st[3]; st[3] = a; a = ln[2]; ln[2] = ln[3]; ln[3] = a; }
    Plan P;
    P.e0 = ln[0] * BLKB; P.e1 = P.e0 + ln[1] * BLKB; P.e2 = P.e1 + ln[2] * BLKB; P.nb = P.e2 + ln[3] * BLKB;
    P.d0 = list + st[0] * BLKB; P.d1 = list + st[1] * BLKB - P.e0; P.d2 = list + st[2] * BLKB - P.e1; P.d3 = list + st[3] * BLKB - P.e2;
    P.nul = nul;
    P.ns = (int)((ln[0] + ln[1] + ln[2] + ln[3] + 3u) >> 2);
    return P;
}
struct Blk { u32x4 l0, l1, oh; };
__device__ __forceinline__ Blk read_blk(const Plan P, u32 vb) {
    const u32 d0 = P.d0, d1 = P.d1, d2 = P.d2, d3 = P.d3, nul = P.nul;
    u32 a = vb < P.e2 ? d2 : d3;
    a = vb < P.e1 ? d1 : a;
    a = vb < P.e0 ? d0 : a;
    a = vb < P.nb ? a + vb : nul;
    const LAS u32x4* p = (const LAS u32x4*)(uintptr_t)a;
    Blk B;
    B.l0 = p[0]; B.l1 = p[1]; B.oh = p[2];
    return B;
}
__device__ __forceinline__ void issue_blk(u32x4 rsrc, const Blk& B, u32 buf, const Lane& L) {
    const bool up = (L.lane & 8) != 0;
    dma4(rsrc, buf, row_off(up ? B.l1[0] : B.l0[0], L.coff[0]), row_off(up ? B.l1[1] : B.l0[1], L.coff[1]),
         row_off(up ? B.l1[2] : B.l0[2], L.coff[2]), row_off(up ? B.l1[3] : B.l0[3], L.coff[3]));
}
__device__ __forceinline__ u32x4 sel_blk(const Blk& B, const Lane& L) {
    u32x4 b;
    b[0] = sel2(B.oh[0], B.l0[0], B.l0[1], L.sh);
    b[1] = sel2(B.oh[2], B.l1[0], B.l1[1], L.sh);
    b[2] = sel2(B.oh[1], B.l0[2], B.l0[3], L.sh);
    b[3] = sel2(B.oh[3], B.l1[2], B.l1[3], L.sh);
    return b;
}
__device__ __forceinline__ void issue_own(u32x4 ra, u32x4 rb, u32 buf, int node0, int lane) {
    const u32 o = (u32)(node0 + (lane >> 3)) * 128u + 4096u;
    const u32 c0 = (u32)(((lane & 7) ^ (lane >> 4)) << 4), c1 = c0 ^ 64u;
    u32 keep;
    asm volatile(
        "s_waitcnt lgkmcnt(0)\n\t"
        "s_mov_b32 %0, m0\n\t"
        "s_mov_b32 m0, %1\n\t"
        "s_nop 0\n\t"
        "buffer_load_dwordx4 %2, %6, 0 offen lds\n\t"
        "buffer_load_dwordx4 %3, %6, 0 offen offset:1024 lds\n\t"
        "buffer_load_dwordx4 %4, %7, 0 offen offset:2048 lds\n\t"
        "buffer_load_dwordx4 %5, %7, 0 offen offset:3072 lds\n\t"
        "s_mov_b32 m0, %0"
        : "=&s"(keep)
        : "s"(buf), "v"(o + c0), "v"(o + c1), "v"(o + c0 - 2048u), "v"(o + c1 - 2048u), "s"(ra), "s"(rb)
        : "memory");
}
template <bool OWN>
__device__ __forceinline__ void gather_group(f32x4 (&acc)[4], u32x4 rsrc, const Plan P, char* ring, const Lane& L,
                                             u32x4 ra, int node0) {
    const u32 rb = lds_addr(ring);
    const int ns = P.ns, nt = ns + (OWN ? 1 : 0);
    u32 vb = (u32)(L.lane >> 4) * BLKB;
    u32x4 bq[2];
    if (PRIO) __builtin_amdgcn_s_setprio(3);
#pragma unroll
    for (int h = 0; h < 2; ++h) {
        const Blk B = read_blk(P, vb);
        vb += 4 * BLKB;
        if (h < ns) issue_blk(rsrc, B, rb + h * 4096, L);
        else if (OWN && h == ns) issue_own(ra, rsrc, rb + h * 4096, node0, L.lane);
        bq[h] = sel_blk(B, L);
    }
    for (int s0 = 0; s0 < ns; s0 += 2) {
        if (PRIO) {
            if (s0 == 2) __builtin_amdgcn_s_setprio(2);
            else if (s0 == 4) __builtin_amdgcn_s_setprio(1);
            else if (s0 == 6) __builtin_amdgcn_s_setprio(0);
        }
#pragma unroll
        for (int h = 0; h < 2; ++h) {
            const int s = s0 + h;
            if (h == 1 && s >= ns) break;
            const Blk B = read_blk(P, vb);
            vb += 4 * BLKB;
            if (s + 1 < nt) WAIT_VM(4); else WAIT_VM(0);
            bf16x8 a[4];
#pragma unroll
            for (int c = 0; c < 4; ++c) a[c] = afrag(ring + h * 4096, L, c);
            if (s + 2 < ns) issue_blk(rsrc, B, rb + h * 4096, L);
            else if (OWN && s + 2 == ns) issue_own(ra, rsrc, rb + h * 4096, node0, L.lane);
#pragma unroll
            for (int c = 0; c < 4; ++c)
                acc[c] = __builtin_amdgcn_mfma_f32_16x16x32_bf16(a[c], __builtin_bit_cast(bf16x8, bq[h]), acc[c], 0, 0, 0);
            bq[h] = sel_blk(B, L);
        }
    }
    if (PRIO) __builtin_amdgcn_s_setprio(0);
    if (OWN) WAIT_VM(0);
}

__device__ __forceinline__ void put_record(char* smem, u32 lo, u32 meta) {
    const u32 sr = ((lo & 0xffffu) * 1342u) >> 24;
    const u32 g = meta >> 4;
    const u32 rank = atomicAdd((u32*)(smem + L_CNT) + g * 4u + sr, 1u);
    if (rank < (u32)CAP2) {
        const u32 slot = (sr >> 1) * CAP2 + ((sr & 1u) ? (u32)(CAP2 - 1) - rank : rank);
        char* base = smem + L_LIST + g * GLB + (slot >> 3) * BLKB;
        *(u32*)(base + (slot & 7u) * 4u) = lo;
        *(u16*)(base + 32 + (slot & 7u) * 2u) = (u16)(1u << (meta & 15u));
    }
}
__device__ __forceinline__ void put_chunk(char* smem, u32x4 v) {
#pragma unroll
    for (int j = 0; j < 3; ++j) {
        const u32 meta = (v[3] >> (8 * j)) & 0xffu;
        if (meta != 0xffu) put_record(smem, v[j], meta);
    }
}
__device__ __forceinline__ void put_surplus(char* smem, u32x2 r, u32 bucket) {
    const u32 g = r[1] >> 4;
    if (bucket_of_group(g) == bucket) put_record(smem, r[0], ((g - (u32)gstart(bucket)) << 4) | (r[1] & 15u));
}
__device__ __forceinline__ void init_lists(char* smem, int t) {
    constexpr int NV = (GPB * GLB + BLKB) / 16;
#pragma unroll
    for (int h = 0; h < 2; ++h) {
        const int i = t + h * 1024;
        if (i < NV + 16) *(u32x4*)(smem + L_LIST + i * 16) = (u32x4)((i < NV && (i % 3) < 2) ? 0xffffffffu : 0u);
    }
}
__device__ __forceinline__ u32 runs_word(const char* smem, int wv) {
    const u32x4 c = *(const u32x4*)(smem + L_CNT + wv * 16);
    const u32 n0 = (c[0] + 7u) >> 3, s1 = max((192u - c[1]) >> 3, n0);
    const u32 e2 = 24u + ((c[2] + 7u) >> 3), s3 = max((384u - c[3]) >> 3, e2);
    return __builtin_amdgcn_readfirstlane(n0 | (s1 << 6) | (e2 << 12) | (s3 << 18));
}

__device__ __forceinline__ void slow_bucket(char* smem, f32x4 (&acc)[4], const u32x4* __restrict__ bl,
                                            const u32x2* __restrict__ ovf, const u32* __restrict__ ovfn, u32 bucket,
                                            u32x4 rsrc, int t, int wv, const Lane& L) {
    int blk = -((NB1 + 7) / 8);
    u32 o0 = 0, n = 0;
    for (;;) {
        if (blk >= 0 && o0 >= n) {
            for (; blk < NB1; ++blk) if ((bl[blk * 8][3] >> 24) == 0u) break;
            if (blk >= NB1) break;
            n = ovfn[blk];
            o0 = 0;
        }
        __syncthreads();
        init_lists(smem, t);
        __syncthreads();
        if (blk < 0) {
            const int c = (blk + (NB1 + 7) / 8) * 64 + t;
            if (t < 64 && c < NB1 * 8) put_chunk(smem, bl[c]);
            ++blk;
        } else {
            if (t < CAP2 && o0 + t < n) put_surplus(smem, ovf[blk * CH + o0 + t], bucket);
            o0 += CAP2;
            if (o0 >= n) ++blk;
        }
        __syncthreads();
        if (wv < GPB) {
            const Plan P = make_plan(runs_word(smem, wv), lds_addr(smem + L_LIST + wv * GLB), lds_addr(smem + L_NUL), 0);
            gather_group<false>(acc, rsrc, P, smem + L_RING + wv * 8192, L, rsrc, 0);
        }
    }
}

__device__ __forceinline__ void store_tile(char* __restrict__ outp, int node0, const f32x4 (&acc)[4], char* buf, int lane) {
    const int n = lane & 15, q = lane >> 4;
#pragma unroll
    for (int c = 0; c < 4; ++c) {
        u32x2 v;
        v[0] = pack_bf16(acc[c][0], acc[c][1]);
        v[1] = pack_bf16(acc[c][2], acc[c][3]);
        *(u32x2*)(buf + n * 128 + (((2 * c + (q >> 1)) ^ (n & 7)) << 4) + 8 * (q & 1)) = v;
    }
    __builtin_amdgcn_wave_barrier();
    char* p = outp + (size_t)node0 * 128 + lane * 16;
    const int r = lane >> 3;
#pragma unroll
    for (int h = 0; h < 2; ++h) st16(p + h * 1024, *(const u32x4*)(buf + h * 1024 + r * 128 + (((lane & 7) ^ r) << 4)));
}

__global__ __launch_bounds__(1024) void k2_sort_spmm(const u32x4* __restrict__ lines, const u32x2* __restrict__ ovf,
                                                     const u32* __restrict__ ovfn, const char* __restrict__ x,
                                                     char* __restrict__ lists, u32* __restrict__ gmask,
                                                     char* __restrict__ t1, const char* __restrict__ w) {
    __shared__ __attribute__((aligned(16))) char smem[L_K2];
    const int t = threadIdx.x, lane = t & 63, wv = __builtin_amdgcn_readfirstlane(t >> 6);
    const int xr = blockIdx.x >> 3;
    const int bucket = (blockIdx.x & 7) * 32 + xr;
    const int g0 = gstart(bucket), ng = gstart(bucket + 1) - g0;
    STAMP(1, 0);
    const u32x4* bl = lines + (size_t)bucket * (NB1 * 8);
    u32x4 v0 = __builtin_nontemporal_load(bl + t), v1 = 0xffffffffu;
    if (t + 1024 < NB1 * 8) v1 = __builtin_nontemporal_load(bl + t + 1024);
    init_lists(smem, t);
    const u32x4 xrs = make_rsrc(x, (u32)NN * 128u);
    __syncthreads();
    asm volatile("" : "+v"(v0), "+v"(v1));
    if (PF2A > 0 && wv < PF2A / 2048)
        touch(xrs, lds_addr(smem + L_JUNK), (u32)(PF1 + (wv * 32 + xr) * 64 + lane) * 128u);
    put_chunk(smem, v0);
    put_chunk(smem, v1);
    {
        unsigned long long f0 = __ballot((t & 7) == 0 && (v0[3] >> 24) == 0u), f1 = __ballot((t & 7) == 0 && (v1[3] >> 24) == 0u);
        while (f0 | f1) {
            int c;
            if (f0) { c = __builtin_ctzll(f0); f0 &= f0 - 1; } else { c = __builtin_ctzll(f1) + 1024; f1 &= f1 - 1; }
            const int blk = (wv * 64 + c) >> 3;
            const u32x2 r0 = ovf[blk * CH + lane];
            const u32 n = ovfn[blk];
            if ((u32)lane < n) put_surplus(smem, r0, bucket);
            for (u32 o = 64 + lane; o < n; o += 64) put_surplus(smem, ovf[blk * CH + o], bucket);
        }
    }
    STAMP(1, 1);
    __syncthreads();
    STAMP(1, 2);
    const Lane L = make_lane(lane);
    const u32x4 rsrc = make_rsrc(x - 4096, (u32)NN * 128u + 4096u);
    f32x4 acc[4];
#pragma unroll
    for (int c = 0; c < 4; ++c) acc[c] = 0.f;
    bool slow;
    {
        const u32x2 c = *(const u32x2*)(smem + L_CNT + (lane & 31) * 8);
        slow = __any(lane < 2 * GPB && c[0] + c[1] > (u32)CAP2);
    }
    if (!slow) {
        if (wv >= GPB) {
            char* gl = lists + (size_t)g0 * GLB;
            const int n16 = ng * (GLB / 16), h = wv - GPB;
            for (int i = h * 64 + lane; i < n16; i += 192) st16(gl + i * 16, *(const u32x4*)(smem + L_LIST + i * 16));
            if (PF2C) {
                WAIT_VM(0);
                const unsigned long long t0 = __builtin_amdgcn_s_memrealtime();
                while (__builtin_amdgcn_s_memrealtime() - t0 < (unsigned long long)PF2C) __builtin_amdgcn_s_sleep(8);
                const u32x4 lrs = make_rsrc(gl, (u32)(ng * GLB));
                for (int i = 0; i < 2; ++i) touch(lrs, lds_addr(smem + L_JUNK), (u32)(((i * 8 + (lane >> 3)) * 3 + h) * 1024 + (lane & 7) * 128));
                const u32x4 ors = make_rsrc(x + (size_t)g0 * 2048, (u32)ng * 2048u);
                touch(ors, lds_addr(smem + L_JUNK), (u32)(h * 64 + lane) * 128u);
                if (h == 0) touch(ors, lds_addr(smem + L_JUNK), (u32)(192 + lane) * 128u);
                if (h == 1 && xr < 3) touch(make_rsrc(w, 192u * 128u), lds_addr(smem + L_JUNK), (u32)(xr * 64 + lane) * 128u);
            }
            STAMP(1, 6);
            return;
        }
        if (wv >= ng) return;
        const u32 word = runs_word(smem, wv);
        const int rot = ORD2 == 0 ? 0 : ORD2 == 1 ? (wv + xr) % 3 : ORD2 == 2 ? ((wv + xr) & 3) : ((wv + xr) & 1);
        const Plan P = make_plan(word, lds_addr(smem + L_LIST + wv * GLB), lds_addr(smem + L_NUL), rot);
        gather_group<false>(acc, rsrc, P, smem + L_RING + wv * 8192, L, rsrc, 0);
        if (lane == 0) gmask[g0 + wv] = word;
    } else {
        slow_bucket(smem, acc, bl, ovf, ovfn, bucket, rsrc, t, wv, L);
        if (wv >= ng) return;
        if (lane == 0) gmask[g0 + wv] = 0xffffffffu;
    }
    STAMP(1, 4);
    store_tile(t1, (g0 + wv) * 16, acc, smem + L_RING + wv * 8192, lane);
}

__global__ __launch_bounds__(1024) void k3_spmm_gemm(const u32x4* __restrict__ lines, const u32x2* __restrict__ ovf,
                                                     const u32* __restrict__ ovfn, const char* __restrict__ x,
                                                     const char* __restrict__ lists, const u32* __restrict__ gmask,
                                                     const char* __restrict__ t1, const char* __restrict__ w,
                                                     char* __restrict__ out) {
    __shared__ __attribute__((aligned(16))) char smem[L_K3];
    const int t = threadIdx.x, lane = t & 63, wv = __builtin_amdgcn_readfirstlane(t >> 6);
    const int xr = blockIdx.x >> 3;
    const int bucket = (blockIdx.x & 7) * 32 + xr;
    const int g0 = gstart(bucket), ng = gstart(bucket + 1) - g0;
    STAMP(2, 0);
    const bool active = wv < ng;
    const int g = active ? g0 + wv : g0;
    const Lane L = make_lane(lane);
    char* list = smem + L_LIST + wv * GLB;
    char* ring = smem + L_RING + wv * 8192;
    const u32 mk = gmask[g];
    if (wv < GPB) {
        const u32x4 lrs = make_rsrc(lists, (u32)(NG * GLB));
        dma1(lrs, lds_addr(list), (u32)(g * GLB + lane * 16));
        dma1(lrs, lds_addr(list + 1024), (u32)(g * GLB + 1024 + lane * 16));
        if (lane < 16) dma1(lrs, lds_addr(list + 2048), (u32)(g * GLB + 2048 + lane * 16));
    } else {
        const u32x4 wrs = make_rsrc(w - 4096, 3u * 64u * 128u + 4096u);
        const int a = lane >> 3;
#pragma unroll
        for (int h = 0; h < 2; ++h) {
            const int ks = (wv - GPB) * 2 + h;
            const u32 rb = (u32)(32 * ks + 4 * (a >> 1) + 2 * (a & 1)) * 128u;
            dma4(wrs, lds_addr(smem + L_W + ks * 4096), rb + L.coff[0], rb + 128u + L.coff[1], rb + 2048u + L.coff[2], rb + 2176u + L.coff[3]);
        }
        if (PF3 > 0) {
            const u32x4 trs = make_rsrc(t1, (u32)PF3 * 128u);
            touch(trs, lds_addr(smem + L_JUNK), (u32)(((wv - GPB) * 32 + xr) * 64 + lane) * 128u);
        }
        if (wv == GPB && lane < 3) *(u32x4*)(smem + L_NUL + lane * 16) = (u32x4)(lane < 2 ? 0xffffffffu : 0u);
    }
    const u32 mku = __builtin_amdgcn_readfirstlane(mk);
    const bool slow = mku == 0xffffffffu;
    if (PF3 > 0 && wv >= GPB) WAIT_VM(1); else WAIT_VM(0);
    STAMP(2, 1);
    __syncthreads();
    const u32x4 rsrc = make_rsrc(t1 - 4096, (u32)NN * 128u + 4096u);
    const u32x4 xrs = make_rsrc(x - 4096, (u32)NN * 128u + 4096u);
    f32x4 acc[4];
#pragma unroll
    for (int c = 0; c < 4; ++c) acc[c] = 0.f;
    STAMP(2, 2);
    CSTAMP(0);
    int ob = 0;
    if (!slow) {
        if (!active) return;
        const Plan P = make_plan(mku, lds_addr(list), lds_addr(smem + L_NUL), 0);
        gather_group<true>(acc, rsrc, P, ring, L, xrs, g * 16);
        ob = (P.ns & 1) * 4096;
    } else {
        slow_bucket(smem, acc, lines + (size_t)bucket * (NB1 * 8), ovf, ovfn, bucket, rsrc, t, wv, L);
        if (!active) return;
        issue_own(xrs, rsrc, lds_addr(ring), g * 16, lane);
        WAIT_VM(0);
    }
    STAMP(2, 3);
    CSTAMP(1);
    u32x2 xf[4], tf[4];
    {
        const int n = lane & 15, q = lane >> 4;
#pragma unroll
        for (int c = 0; c < 4; ++c) {
            const char* p = ring + ob + n * 128 + (((2 * c + (q >> 1)) ^ (n >> 1)) << 4) + 8 * (q & 1);
            xf[c] = *(const u32x2*)p;
            tf[c] = *(const u32x2*)(p + 2048);
        }
    }
    f32x4 o[4];
#pragma unroll
    for (int c = 0; c < 4; ++c) o[c] = 0.f;
#pragma unroll
    for (int kk = 0; kk < 2; ++kk) {
        u32x4 bx, bt, b2;
        bx[0] = xf[2 * kk][0]; bx[1] = xf[2 * kk][1]; bx[2] = xf[2 * kk + 1][0]; bx[3] = xf[2 * kk + 1][1];
        bt[0] = tf[2 * kk][0]; bt[1] = tf[2 * kk][1]; bt[2] = tf[2 * kk + 1][0]; bt[3] = tf[2 * kk + 1][1];
#pragma unroll
        for (int h = 0; h < 2; ++h) {
            const int c = 2 * kk + h;
            b2[2 * h] = pack_bf16(2.f * acc[c][0] - bf_lo(xf[c][0]), 2.f * acc[c][1] - bf_hi(xf[c][0]));
            b2[2 * h + 1] = pack_bf16(2.f * acc[c][2] - bf_lo(xf[c][1]), 2.f * acc[c][3] - bf_hi(xf[c][1]));
        }
#pragma unroll
        for (int c = 0; c < 4; ++c) {
            o[c] = __builtin_amdgcn_mfma_f32_16x16x32_bf16(afrag(smem + L_W + kk * 4096, L, c), __builtin_bit_cast(bf16x8, bx), o[c], 0, 0, 0);
            o[c] = __builtin_amdgcn_mfma_f32_16x16x32_bf16(afrag(smem + L_W + (2 + kk) * 4096, L, c), __builtin_bit_cast(bf16x8, bt), o[c], 0, 0, 0);
            o[c] = __builtin_amdgcn_mfma_f32_16x16x32_bf16(afrag(smem + L_W + (4 + kk) * 4096, L, c), __builtin_bit_cast(bf16x8, b2), o[c], 0, 0, 0);
        }
    }
    store_tile(out, g * 16, o, ring + (ob ^ 4096), lane);
}

extern "C" void kernel_launch(void* const* d_in, const int* in_sizes, int n_in, void* d_out, int out_size, void* d_ws,
                              size_t ws_size, hipStream_t stream) {
    const char* x = (const char*)d_in[0];
    const int* src = (const int*)d_in[1];
    const int* dst = (const int*)d_in[2];
    const void* ew = d_in[3];
    const char* w = (const char*)d_in[4];
    char* ws = (char*)d_ws;
    k1_partition<<<NB1, 1024, 0, stream>>>((const i32x4*)src, (const i32x4*)dst, (const u32x2*)ew, ws + W_LINE,
                                           (u32x2*)(ws + W_OVF), (u32*)(ws + W_OVFN), x);
    k2_sort_spmm<<<NBK, 1024, 0, stream>>>((const u32x4*)(ws + W_LINE), (const u32x2*)(ws + W_OVF), (const u32*)(ws + W_OVFN),
                                           x, ws + W_LIST, (u32*)(ws + W_GMSK), ws + W_T1, w);
    k3_spmm_gemm<<<NBK, 1024, 0, stream>>>((const u32x4*)(ws + W_LINE), (const u32x2*)(ws + W_OVF), (const u32*)(ws + W_OVFN),
                                           x, ws + W_LIST, (const u32*)(ws + W_GMSK), ws + W_T1, w, (char*)d_out);
}
```

```cpp
#include <hip/hip_runtime.h>
#include <hip/hip_bf16.h>
#include <stdint.h>

typedef unsigned int u32;
typedef unsigned short u16;
typedef unsigned char u8;
typedef u32 u32x4 __attribute__((ext_vector_type(4)));
typedef u32 u32x2 __attribute__((ext_vector_type(2)));
typedef int i32x4 __attribute__((ext_vector_type(4)));
typedef short s16x2 __attribute__((ext_vector_type(2)));
typedef short s16x4 __attribute__((ext_vector_type(4)));
typedef __bf16 bf16x8 __attribute__((ext_vector_type(8)));
typedef __bf16 bf16x2 __attribute__((ext_vector_type(2)));
typedef float f32x4 __attribute__((ext_vector_type(4)));
#define LAS __attribute__((address_space(3)))

constexpr int NN = 50000, NE = 800000;
constexpr int NBK = 256;
constexpr int NB1 = 250, CH = 3200;
constexpr int GPB = 13;
constexpr int SLOTS = 24;
constexpr int CAP2 = 192;

#define PF1 0
#define PF2A 1
#define PF3 0
#define TSEQ 0
#define XROT 1
#define ORD2 1
#define PRIO 2
#define ACQ 0
#define NSH 8
#define NREP 32
#define POLLPIPE 0
#define NTL 0
#define LDNT(p) (*(p))
#define POLLSLEEP 2

constexpr int L_LO = 0;
constexpr int L_NUL = L_LO + GPB * 2 * CAP2 * 4;
constexpr int L_DUMP = L_NUL + 32;
constexpr int L_OH = L_DUMP + 32;
constexpr int L_CNT = L_OH + L_OH / 2;
constexpr int L_JUNK = L_CNT + 256;
constexpr int L_TAB = L_JUNK + 256;
constexpr int L_W = (L_TAB + GPB * 128 + 255) / 256 * 256;
constexpr int L_RING = L_W + 24576;
constexpr int L_K2 = L_RING + GPB * 8192;
static_assert(L_K2 <= 163840, "lds");
static_assert(L_OH % 32 == 0 && L_CNT % 16 == 0 && L_TAB % 16 == 0, "align");

constexpr size_t W_LINE = 0;
constexpr size_t W_OVF = W_LINE + (size_t)NBK * NB1 * 128;
constexpr size_t W_OVFN = W_OVF + (size_t)NB1 * CH * 8;
constexpr size_t W_BAR = W_OVFN + 1024;
constexpr size_t W_T1 = W_BAR + 1024 * 256;
static_assert(W_OVF % 128 == 0 && W_BAR % 256 == 0 && W_T1 % 128 == 0 && NREP * NSH <= 512, "align");

__device__ __forceinline__ u32 lds_addr(const void* p) { return (u32)(uintptr_t)(const LAS char*)p; }
__device__ __forceinline__ u32 bstart(u32 b) { return (b * 3125u) >> 4; }
__device__ __forceinline__ u32 bucket_of(u32 d) { return __umulhi(d * 16u + 15u, 1374390u); }
__device__ __forceinline__ u32 make_key(u32 local, u32 src) {
    return ((local >> 4) << 2) + 4u + ((src * 1342u) >> 24) + ((local & 15u) << 6);
}

__device__ __forceinline__ u32x4 make_rsrc(const void* base, u32 bytes) {
    const uint64_t a = (uint64_t)base;
    u32x4 r;
    r[0] = (u32)a;
    r[1] = (u32)(a >> 32) & 0xffffu;
    r[2] = bytes;
    r[3] = 0x00020000u;
    return r;
}

template <bool SC1>
__device__ __forceinline__ void dma4(u32x4 rsrc, u32 lds, u32 o0, u32 o1, u32 o2, u32 o3) {
    u32 keep;
    if (SC1)
        asm volatile(
            "s_waitcnt lgkmcnt(0)\n\t"
            "s_mov_b32 %0, m0\n\t"
            "s_mov_b32 m0, %1\n\t"
            "s_nop 0\n\t"
            "buffer_load_dwordx4 %2, %6, 0 offen sc1 lds\n\t"
            "buffer_load_dwordx4 %3, %6, 0 offen offset:1024 sc1 lds\n\t"
            "buffer_load_dwordx4 %4, %6, 0 offen offset:2048 sc1 lds\n\t"
            "buffer_load_dwordx4 %5, %6, 0 offen offset:3072 sc1 lds\n\t"
            "s_mov_b32 m0, %0"
            : "=&s"(keep)
            : "s"(lds), "v"(o0), "v"(o1), "v"(o2), "v"(o3), "s"(rsrc)
            : "memory");
    else
        asm volatile(
            "s_waitcnt lgkmcnt(0)\n\t"
            "s_mov_b32 %0, m0\n\t"
            "s_mov_b32 m0, %1\n\t"
            "s_nop 0\n\t"
            "buffer_load_dwordx4 %2, %6, 0 offen lds\n\t"
            "buffer_load_dwordx4 %3, %6, 0 offen offset:1024 lds\n\t"
            "buffer_load_dwordx4 %4, %6, 0 offen offset:2048 lds\n\t"
            "buffer_load_dwordx4 %5, %6, 0 offen offset:3072 lds\n\t"
            "s_mov_b32 m0, %0"
            : "=&s"(keep)
            : "s"(lds), "v"(o0), "v"(o1), "v"(o2), "v"(o3), "s"(rsrc)
            : "memory");
}
__device__ __forceinline__ void dma2(u32x4 rsrc, u32 lds, u32 o0, u32 o1) {
    u32 keep;
    asm volatile(
        "s_mov_b32 %0, m0\n\t"
        "s_mov_b32 m0, %1\n\t"
        "s_nop 0\n\t"
        "buffer_load_dwordx4 %2, %4, 0 offen lds\n\t"
        "buffer_load_dwordx4 %3, %4, 0 offen offset:1024 lds\n\t"
        "s_mov_b32 m0, %0"
        : "=&s"(keep)
        : "s"(lds), "v"(o0), "v"(o1), "s"(rsrc)
        : "memory");
}
__device__ __forceinline__ void touch(u32x4 rsrc, u32 junk_lds, u32 off) {
    u32 keep;
    asm volatile(
        "s_mov_b32 %0, m0\n\t"
        "s_mov_b32 m0, %1\n\t"
        "s_nop 0\n\t"
        "buffer_load_dword %2, %3, 0 offen sc1 lds\n\t"
        "s_mov_b32 m0, %0"
        : "=&s"(keep)
        : "s"(junk_lds), "v"(off), "s"(rsrc)
        : "memory");
}
__device__ __forceinline__ u32 walk_row(u32 j, u32 p) {
    const u32 r = (j >> 1) * 12500u + p;
    return r >= (u32)NN ? r - (u32)NN : r;
}
#define WAIT_VM(n) asm volatile("s_waitcnt vmcnt(" #n ")" ::: "memory")
__device__ __forceinline__ void st16(void* p, u32x4 v) {
    asm volatile("global_store_dwordx4 %0, %1, off sc1\n\ts_nop 1" ::"v"(p), "v"(v) : "memory");
}

#define STAMP_DECL do {} while (0)
#define STAMP(k, i) do {} while (0)
#define STAMP_FLUSH(k) do {} while (0)

__device__ __forceinline__ u32 pack_bf16(float a, float b) {
    bf16x2 v;
    v[0] = (__bf16)a;
    v[1] = (__bf16)b;
    return __builtin_bit_cast(u32, v);
}
__device__ __forceinline__ float bf_lo(u32 v) { return __uint_as_float(v << 16); }
__device__ __forceinline__ float bf_hi(u32 v) { return __uint_as_float(v & 0xffff0000u); }

__global__ __launch_bounds__(1024) void k1_partition(const i32x4* __restrict__ src4, const i32x4* __restrict__ dst4,
                                                     const u32x2* __restrict__ ew4, char* __restrict__ lines,
                                                     u32x2* __restrict__ ovf, u32* __restrict__ ovfn,
                                                     u32* __restrict__ bar, const char* __restrict__ x) {
    __shared__ __attribute__((aligned(16))) u32 stage[4 * 2048];
    __shared__ __attribute__((aligned(16))) u32 cnt[256 + 64 + 4];
    const int t = threadIdx.x, blk = blockIdx.x;
    STAMP_DECL;
    STAMP(0, 0);
    const int e4 = blk * (CH / 4) + min(t, CH / 4 - 1);
    i32x4 d = LDNT(dst4 + e4);
    i32x4 s = LDNT(src4 + e4);
    u32x2 w = LDNT(ew4 + e4);
    if (t < 512) *(u32x4*)&stage[3 * 2048 + t * 4] = (u32x4)0u;
    else if (t < 512 + 81) *(u32x4*)&cnt[(t - 512) * 4] = (u32x4)0u;
    __syncthreads();
    if (t < CH / 4) {
        u32 rk[4], bk[4];
#pragma unroll
        for (int i = 0; i < 4; ++i) {
            bk[i] = bucket_of((u32)d[i]);
            rk[i] = atomicAdd(&cnt[bk[i]], 1u);
        }
#pragma unroll
        for (int i = 0; i < 4; ++i) {
            const u32 key = make_key((u32)d[i] - bstart(bk[i]), (u32)s[i]);
            const u32 lo = (u32)s[i] | ((i & 1) ? (w[i >> 1] & 0xffff0000u) : (w[i >> 1] << 16));
            const u32 c = bk[i] * 8u + (rk[i] & 7u), sub = rk[i] >> 3;
            if (rk[i] < (u32)SLOTS) {
                stage[sub * 2048u + c] = lo;
                atomicOr(&stage[3 * 2048 + c], key << (2u + 10u * sub));
            } else {
                const u32 o = atomicAdd(&cnt[320], 1u);
                u32x2 r;
                r[0] = lo; r[1] = (u32)d[i];
                ovf[blk * CH + o] = r;
                atomicOr(&stage[3 * 2048 + bk[i] * 8u], 1u);
            }
        }
    }
    if (PF1 > 0 && t >= 832) {
        asm volatile("" : "+v"(w));
        const u32x4 xrs = make_rsrc(x, (u32)NN * 128u);
#pragma unroll
        for (int i = 0; i < PF1; ++i)
            touch(xrs, lds_addr(&cnt[256]), walk_row(blk & 7, (u32)(i * 6144 + (blk >> 3) * 192 + (t - 832))) * 128u);
    }
    STAMP(0, 1);
    __syncthreads();
    STAMP(0, 2);
#pragma unroll
    for (int h = 0; h < 2; ++h) {
        const int c = t + h * 1024;
        u32x4 v;
        v[0] = stage[c]; v[1] = stage[2048 + c]; v[2] = stage[4096 + c]; v[3] = stage[6144 + c];
        st16(lines + ((size_t)(c >> 3) * NB1 + blk) * 128 + (c & 7) * 16, v);
    }
    if (t == 0) ovfn[blk] = cnt[320];
    if (blk < NREP && t >= 1024 - NSH) bar[(blk * NSH + (1023 - t)) * 64] = 0u;
    if (PF3 > 0 && blk >= 64 && blk < 96 && t < 64) bar[(512 + (blk - 64) * 8 + (t >> 3)) * 64 + (t & 7)] = 0u;
    STAMP(0, 4);
}

struct Lane {
    u32 coff[4];
    u32 tra[4];
    u32 sh;
    int lane;
};
__device__ __forceinline__ Lane make_lane(int lane) {
    Lane L;
    L.lane = lane;
    const int a = lane >> 3, p = lane & 7;
#pragma unroll
    for (int i = 0; i < 4; ++i) L.coff[i] = (u32)(((p ^ ((((a >> 1) & 1) << 1) | ((i & 1) << 2))) << 4) + 4096 - 1024 * i);
    const int g = lane >> 4, q = (lane & 15) >> 2, pp = lane & 3;
    const int sw = ((g & 1) << 1) | ((q & 1) << 2);
#pragma unroll
    for (int c = 0; c < 4; ++c)
        L.tra[c] = (u32)(128 * (8 * (q & 1) + 2 * g + (q >> 1)) + 16 * ((2 * c + (pp >> 1)) ^ sw) + 8 * (pp & 1));
    L.sh = (u32)(15 - (lane & 15)) * 0x10001u;
    return L;
}
__device__ __forceinline__ bf16x8 afrag(const char* buf, const Lane& L, int c) {
    const s16x4 a0 = __builtin_amdgcn_ds_read_tr16_b64_v4i16((LAS s16x4*)(buf + L.tra[c]));
    const s16x4 a1 = __builtin_amdgcn_ds_read_tr16_b64_v4i16((LAS s16x4*)(buf + L.tra[c] + 2048));
    return __builtin_bit_cast(bf16x8, __builtin_shufflevector(a0, a1, 0, 1, 2, 3, 4, 5, 6, 7));
}
__device__ __forceinline__ u32 row_off(u32 lo, u32 add) {
    u32 r;
    asm("v_mad_u32_u16 %0, %1, %2, %3" : "=v"(r) : "v"(lo), "s"(128u), "v"(add));
    return r;
}
__device__ __forceinline__ u32 sel2(u32 oh, u32 lo0, u32 lo1, u32 sh) {
    const s16x2 m = (__builtin_bit_cast(s16x2, oh) << __builtin_bit_cast(s16x2, sh)) >> 15;
    return __builtin_amdgcn_perm(lo1, lo0, 0x07060302u) & __builtin_bit_cast(u32, m);
}
__device__ __forceinline__ int make_tab(char* smem, int wv, int lane, int q0, int rot) {
    const u32x4 cv = *(const u32x4*)(smem + L_CNT + 16 + wv * 16);
    const u32 c0 = __builtin_amdgcn_readfirstlane(cv[0]), c1 = __builtin_amdgcn_readfirstlane(cv[1]);
    const u32 c2 = __builtin_amdgcn_readfirstlane(cv[2]), c3 = __builtin_amdgcn_readfirstlane(cv[3]);
    const u32 b0 = (u32)wv * 48u;
    const u32 n0 = (c0 + 31u) >> 5, s1 = max((c1 + 4u) >> 5, n0);
    const u32 e2 = (c2 + 31u) >> 5, s3 = max((c3 + 4u) >> 5, e2);
    u32 sa = b0, sb = s1, sc = b0 + 24u, sd = s3, la = n0 - b0, lb = b0 + 24u - s1, lc = e2 - sc, ld = b0 + 48u - s3, u;
    if (q0 & 1) { u = sa; sa = sb; sb = sc; sc = sd; sd = u; u = la; la = lb; lb = lc; lc = ld; ld = u; }
    if (q0 & 2) { u = sa; sa = sc; sc = u; u = sb; sb = sd; sd = u; u = la; la = lc; lc = u; u = lb; lb = ld; ld = u; }
    if (rot >= 1) { u = sa; sa = sb; sb = u; u = la; la = lb; lb = u; }
    if (rot >= 2) { u = sb; sb = sc; sc = u; u = lb; lb = lc; lc = u; }
    if (rot >= 3) { u = sc; sc = sd; sd = u; u = lc; lc = ld; ld = u; }
    const u32 ea = la, eb = ea + lb, ec = eb + lc, nb = ec + ld;
    const int ns = (int)((nb + 3u) >> 2);
    const int k = min(min(max(TSEQ - ns, 0), 2), max(ns - 1, 0));
    u32 v = (u32)lane;
    bool hole = false;
    if (k >= 2) { hole = (v >> 2) == 3u; v = v >= 16u ? v - 4u : v; }
    if (k >= 1) { hole = hole || (v >> 2) == 1u; v = v >= 8u ? v - 4u : v; }
    u32 pb = v < ec ? sc - eb : sd - ec;
    pb = v < eb ? sb - ea : pb;
    pb = v < ea ? sa : pb;
    const u32 a = (v < nb && !hole) ? (pb + v) * 32u : (u32)L_NUL;
    *(u16*)(smem + L_TAB + wv * 128 + lane * 2) = (u16)a;
    __builtin_amdgcn_wave_barrier();
    return (int)(((1u << (ns + k)) - 1u) & ~(k >= 2 ? 10u : k >= 1 ? 2u : 0u));
}
struct Blk { u32x4 ix, l0, l1, oh; };
__device__ __forceinline__ Blk read_blk(const char* smem, u32 a, const Lane& L) {
    Blk B;
    B.ix = *(const u32x4*)(smem + a + (L.lane & 8) * 2);
    B.l0 = *(const u32x4*)(smem + a);
    B.l1 = *(const u32x4*)(smem + a + 16);
    B.oh = *(const u32x4*)(smem + L_OH + (a >> 1));
    return B;
}
template <bool SC1>
__device__ __forceinline__ void issue_blk(u32x4 rsrc, const Blk& B, u32 buf, const Lane& L) {
    dma4<SC1>(rsrc, buf, row_off(B.ix[0], L.coff[0]), row_off(B.ix[1], L.coff[1]), row_off(B.ix[2], L.coff[2]), row_off(B.ix[3], L.coff[3]));
}
__device__ __forceinline__ u32x4 sel_blk(const Blk& B, const Lane& L) {
    u32x4 b;
    b[0] = sel2(B.oh[0], B.l0[0], B.l0[1], L.sh);
    b[1] = sel2(B.oh[2], B.l1[0], B.l1[1], L.sh);
    b[2] = sel2(B.oh[1], B.l0[2], B.l0[3], L.sh);
    b[3] = sel2(B.oh[3], B.l1[2], B.l1[3], L.sh);
    return b;
}
struct Prime { u32x4 off[2], bq[2]; u32 an; };
__device__ __forceinline__ Prime make_prime(const char* smem, int wv, const Lane& L) {
    const u16* tp = (const u16*)(smem + L_TAB + wv * 128) + (L.lane >> 4);
    Prime P;
#pragma unroll
    for (int h = 0; h < 2; ++h) {
        const Blk B = read_blk(smem, tp[4 * h], L);
#pragma unroll
        for (int i = 0; i < 4; ++i) P.off[h][i] = row_off(B.ix[i], L.coff[i]);
        P.bq[h] = sel_blk(B, L);
    }
    P.an = tp[8];
    return P;
}
template <bool SC1>
__device__ __forceinline__ void gather_group(f32x4 (&acc)[4], u32x4 rsrc, char* smem, int wv, int ns, const Lane& L, const Prime& P) {
    char* ring = smem + L_RING + wv * 8192;
    const u32 rb = lds_addr(ring);
    const u16* tp = (const u16*)(smem + L_TAB + wv * 128) + (L.lane >> 4);
    u32x4 bq[2] = {P.bq[0], P.bq[1]};
    u32 m = (u32)ns;
    if (PRIO) __builtin_amdgcn_s_setprio(3);
#pragma unroll
    for (int h = 0; h < 2; ++h)
        if (m >> h & 1u) dma4<SC1>(rsrc, rb + h * 4096, P.off[h][0], P.off[h][1], P.off[h][2], P.off[h][3]);
    u32 an = P.an;
    while (m) {
#pragma unroll
        for (int h = 0; h < 2; ++h) {
            if (h == 1 && m == 0u) break;
            if (PRIO == 2) {
                if (m == 127u) __builtin_amdgcn_s_setprio(2);
                else if (m == 31u) __builtin_amdgcn_s_setprio(1);
                else if (m == 7u) __builtin_amdgcn_s_setprio(0);
            }
            const Blk B = read_blk(smem, an, L);
            an = tp[4 * (h + 3)];
            if (h == 0 || (m & 1u)) {
                if (m & 2u) WAIT_VM(4); else WAIT_VM(0);
                bf16x8 a[4];
#pragma unroll
                for (int c = 0; c < 4; ++c) a[c] = afrag(ring + h * 4096, L, c);
                if (m & 4u) issue_blk<SC1>(rsrc, B, rb + h * 4096, L);
#pragma unroll
                for (int c = 0; c < 4; ++c)
                    acc[c] = __builtin_amdgcn_mfma_f32_16x16x32_bf16(a[c], __builtin_bit_cast(bf16x8, bq[h]), acc[c], 0, 0, 0);
            } else if (m & 4u) {
                issue_blk<SC1>(rsrc, B, rb + h * 4096, L);
            }
            bq[h] = sel_blk(B, L);
            m >>= 1;
        }
        tp += 8;
    }
    if (PRIO) __builtin_amdgcn_s_setprio(0);
}

__device__ __forceinline__ void put_record(char* smem, u32 lo, u32 u) {
    const u32 dir = ((u32)__builtin_amdgcn_sbfe(u, 2, 1) << 3) | 4u;
    u32 a = atomicAdd((u32*)(smem + L_CNT + (u & 0xfcu)), dir);
    a = min(a, (u32)L_DUMP);
    *(u32*)(smem + a) = lo;
    *(u16*)(smem + L_OH + (a >> 1)) = (u16)(1u << ((u >> 8) & 15u));
}
struct Pos { u32 a[3], oh[3]; bool ok[3]; };
__device__ __forceinline__ Pos claim_chunk(char* smem, u32 keys) {
    Pos P;
#pragma unroll
    for (int k = 0; k < 3; ++k) {
        const u32 u = keys >> (10 * k);
        const u32 ca = u & 0xfcu;
        u32 dir;
        asm("v_bfe_i32 %0, %1, 2, 1\n\tv_lshl_or_b32 %0, %0, 3, 4" : "=v"(dir) : "v"(u));
        asm("v_lshlrev_b16_sdwa %0, %1, %2 dst_sel:DWORD dst_unused:UNUSED_PAD src0_sel:BYTE_1 src1_sel:DWORD" : "=v"(P.oh[k]) : "v"(u), "v"(1u));
        P.ok[k] = ca != 0u;
        P.a[k] = dir;
        if (P.ok[k]) P.a[k] = atomicAdd((u32*)(smem + L_CNT + ca), dir);
    }
    return P;
}
__device__ __forceinline__ void write_chunk(char* smem, Pos& P, u32x4 v) {
    asm volatile("" : "+v"(P.a[0]), "+v"(P.a[1]), "+v"(P.a[2]));
#pragma unroll
    for (int k = 0; k < 3; ++k) {
        const u32 p = min(P.a[k], (u32)L_DUMP);
        if (P.ok[k]) {
            *(u32*)(smem + p) = v[k];
            *(u16*)(smem + L_OH + (p >> 1)) = (u16)P.oh[k];
        }
    }
}
__device__ __forceinline__ void put_surplus(char* smem, u32x2 r, u32 bucket) {
    if (bucket_of(r[1]) == bucket) put_record(smem, r[0], make_key(r[1] - bstart(bucket), r[0] & 0xffffu) << 2);
}
template <bool FIRST>
__device__ __forceinline__ void init_lists(char* smem, int t) {
#pragma unroll
    for (int h = 0; h < 2; ++h) {
        const int i = t + h * 1024;
        if (i < L_CNT / 16) *(u32x4*)(smem + i * 16) = (u32x4)(i < L_OH / 16 ? 0xffffffffu : 0u);
    }
    if (t >= 960) {
        const u32 b = (u32)t - 960u - 4u;
        if (b < 4u * GPB) *(u32*)(smem + L_CNT + 16 + b * 4) = (b >> 1) * (CAP2 * 4u) + ((b & 1u) ? (CAP2 - 1) * 4u : 0u);
        else if (FIRST) *(u32*)(smem + L_CNT + 16 + (int)b * 4) = 0u;
    }
}

template <bool SC1>
__device__ __forceinline__ void slow_bucket(char* smem, f32x4 (&acc)[4], const u32x4* __restrict__ bl,
                                            const u32x2* __restrict__ ovf, const u32* __restrict__ ovfn, u32 bucket,
                                            u32x4 rsrc, int t, int wv, const Lane& L) {
    int blk = -((NB1 + 7) / 8);
    u32 o0 = 0, n = 0;
    for (;;) {
        if (blk >= 0 && o0 >= n) {
            for (; blk < NB1; ++blk) if (bl[blk * 8][3] & 1u) break;
            if (blk >= NB1) break;
            n = ovfn[blk];
            o0 = 0;
        }
        __syncthreads();
        init_lists<false>(smem, t);
        __syncthreads();
        if (blk < 0) {
            const int c = (blk + (NB1 + 7) / 8) * 64 + t;
            if (t < 64 && c < NB1 * 8) { const u32x4 v = bl[c]; Pos P = claim_chunk(smem, v[3]); write_chunk(smem, P, v); }
            ++blk;
        } else {
            if (t < CAP2 && o0 + t < n) put_surplus(smem, ovf[blk * CH + o0 + t], bucket);
            o0 += CAP2;
            if (o0 >= n) ++blk;
        }
        __syncthreads();
        if (wv < GPB) {
            const int ns = make_tab(smem, wv, L.lane, 0, 0);
            gather_group<SC1>(acc, rsrc, smem, wv, ns, L, make_prime(smem, wv, L));
        }
    }
}

__device__ __forceinline__ void store_tile(char* __restrict__ outp, int node0, int nrows, const u32x2 (&v)[4], char* buf, int lane) {
    const int n = lane & 15, q = lane >> 4;
#pragma unroll
    for (int c = 0; c < 4; ++c) *(u32x2*)(buf + n * 128 + (((2 * c + (q >> 1)) ^ (n & 7)) << 4) + 8 * (q & 1)) = v[c];
    __builtin_amdgcn_wave_barrier();
    char* p = outp + (size_t)node0 * 128 + lane * 16;
    const int r = lane >> 3;
#pragma unroll
    for (int h = 0; h < 2; ++h)
        if (r + 8 * h < nrows) st16(p + h * 1024, *(const u32x4*)(buf + h * 1024 + r * 128 + (((lane & 7) ^ r) << 4)));
}
__device__ __forceinline__ void dense(f32x4 (&o)[4], const char* smem, int k, const u32x2 (&f)[4], const Lane& L) {
#pragma unroll
    for (int kk = 0; kk < 2; ++kk) {
        u32x4 b;
        b[0] = f[2 * kk][0]; b[1] = f[2 * kk][1]; b[2] = f[2 * kk + 1][0]; b[3] = f[2 * kk + 1][1];
#pragma unroll
        for (int c = 0; c < 4; ++c)
            o[c] = __builtin_amdgcn_mfma_f32_16x16x32_bf16(afrag(smem + L_W + (2 * k + kk) * 4096, L, c), __builtin_bit_cast(bf16x8, b), o[c], 0, 0, 0);
    }
}

__global__ __launch_bounds__(1024) void k2_cheb(const u32x4* __restrict__ lines, const u32x2* __restrict__ ovf,
                                                const u32* __restrict__ ovfn, const char* __restrict__ x, u32* bar,
                                                char* t1, const char* __restrict__ w, char* __restrict__ out) {
    __shared__ __attribute__((aligned(256))) char smem[L_K2];
    const int t = threadIdx.x, lane = t & 63, wv = __builtin_amdgcn_readfirstlane(t >> 6);
    const int xr = blockIdx.x >> 3;
    const int bucket = (blockIdx.x & 7) * 32 + xr;
    const bool active = wv < GPB;
    const int node0 = (int)bstart(bucket) + wv * 16;
    const int nrows = min((int)bstart(bucket + 1) - node0, 16);
    char* ring = smem + L_RING + wv * 8192;
    STAMP_DECL;
    STAMP(1, 0);
    const u32x4* bl = lines + (size_t)bucket * (NB1 * 8);
    const u32x4 v0 = LDNT(bl + t);
    u32x4 v1 = LDNT(bl + min(t + 1024, NB1 * 8 - 1));
    init_lists<true>(smem, t);
    const u32x4 xrs = make_rsrc(x, (u32)NN * 128u);
    Lane L = make_lane(lane);
    asm volatile("" : "+v"(L.coff[0]), "+v"(L.coff[1]), "+v"(L.coff[2]), "+v"(L.coff[3]), "+v"(L.tra[0]), "+v"(L.tra[1]),
                 "+v"(L.tra[2]), "+v"(L.tra[3]), "+v"(L.sh));
    __syncthreads();
    __builtin_amdgcn_sched_barrier(0);
    {
        Pos P0 = claim_chunk(smem, v0[3]);
        write_chunk(smem, P0, v0);
    }
    __builtin_amdgcn_sched_barrier(0);
    if (t + 1024 >= NB1 * 8) v1[3] = 0u;
    unsigned long long f0 = __ballot(v0[3] & 1u), f1 = __ballot(v1[3] & 1u);
    STAMP(1, 1);
    u32x2 r0 = 0u;
    u32 n0 = 0;
    if (f0 | f1) {
        const int blk = (wv * 64 + (f0 ? __builtin_ctzll(f0) : __builtin_ctzll(f1) + 1024)) >> 3;
        r0 = ovf[blk * CH + lane];
        n0 = ovfn[blk];
    }
    if (active) {
        const u32 o = (u32)(node0 + (lane >> 3)) * 128u + (u32)(((lane & 7) ^ (lane >> 4)) << 4);
        dma2(xrs, lds_addr(ring), o, o ^ 64u);
    }
    if (PF2A > 0 && wv >= GPB) {
#pragma unroll
        for (int i = 0; i < PF2A; ++i)
            touch(xrs, lds_addr(smem + L_JUNK),
                  (PF1 == 0 && PF2A == 1 ? bstart(bucket ^ 32) + (u32)(t - GPB * 64)
                                         : walk_row(blockIdx.x & 7, (u32)((PF1 + i) * 6144 + xr * 192 + (t - GPB * 64)))) * 128u);
    }
    {
        Pos P1 = claim_chunk(smem, v1[3]);
        write_chunk(smem, P1, v1);
    }
    for (bool first = true; f0 | f1; first = false) {
        int c;
        if (f0) { c = __builtin_ctzll(f0); f0 &= f0 - 1; } else { c = __builtin_ctzll(f1) + 1024; f1 &= f1 - 1; }
        const int blk = (wv * 64 + c) >> 3;
        if (!first) { r0 = ovf[blk * CH + lane]; n0 = ovfn[blk]; }
        if ((u32)lane < n0) put_surplus(smem, r0, bucket);
        for (u32 o = 64 + lane; o < n0; o += 64) put_surplus(smem, ovf[blk * CH + o], bucket);
    }
    __syncthreads();
    STAMP(1, 2);
    bool slow;
    {
        const u32x2 c = *(const u32x2*)(smem + L_CNT + 16 + min(lane, 2 * GPB - 1) * 8);
        slow = __any((int)c[0] > (int)c[1] + 4);
    }
    u32* lflag = (u32*)(smem + L_CNT) + 60;
    if (wv >= GPB) {
        const u32x4 wrs = make_rsrc(w - 4096, 3u * 64u * 128u + 4096u);
        const int a = lane >> 3;
        __builtin_amdgcn_s_setprio(3);
#pragma unroll
        for (int h = 0; h < 2; ++h) {
            const int ks = (wv - GPB) * 2 + h;
            const u32 rb = (u32)(32 * ks + 4 * (a >> 1) + 2 * (a & 1)) * 128u;
            dma4<false>(wrs, lds_addr(smem + L_W + ks * 4096), rb + L.coff[0], rb + 128u + L.coff[1], rb + 2048u + L.coff[2], rb + 2176u + L.coff[3]);
        }
        __builtin_amdgcn_s_setprio(0);
        WAIT_VM(0);
        if (lane == 0) __hip_atomic_fetch_add(lflag + 1, 1u, __ATOMIC_RELAXED, __HIP_MEMORY_SCOPE_WORKGROUP);
    }
    if (PF3 > 0 && wv == GPB && !slow) {
        const u32* fl = bar + (512 + blockIdx.x) * 64;
        const u32x4 trs = make_rsrc(t1, (u32)NN * 128u);
        u32 todo = (1u << PF3) - 1u;
        while (__hip_atomic_load(lflag, __ATOMIC_RELAXED, __HIP_MEMORY_SCOPE_WORKGROUP) == 0u) __builtin_amdgcn_s_sleep(8);
        while (todo && __hip_atomic_load(lflag + 2, __ATOMIC_RELAXED, __HIP_MEMORY_SCOPE_WORKGROUP) == 0u) {
            u32 f = 0;
            if (lane < 8) f = __hip_atomic_load(fl + lane, __ATOMIC_RELAXED, __HIP_MEMORY_SCOPE_AGENT);
            u32 rdy = (u32)__ballot(f != 0u) & todo;
            todo &= ~rdy;
            while (rdy) {
                const u32 k = __builtin_ctz(rdy);
                rdy &= rdy - 1u;
                const u32 b = ((((blockIdx.x & 6u) + k) & 7u) << 5) + xr;
#pragma unroll
                for (int i = 0; i < 3; ++i) touch(trs, lds_addr(smem + L_JUNK), (bstart(b) + (u32)(i * 64 + lane)) * 128u);
            }
            __builtin_amdgcn_s_sleep(2);
        }
    }
    if (!slow && !active) return;
    u32x2 xf[4];
    WAIT_VM(0);
    if (wv < GPB) {
        const int n = lane & 15, q = lane >> 4;
#pragma unroll
        for (int c = 0; c < 4; ++c) xf[c] = *(const u32x2*)(ring + n * 128 + (((2 * c + (q >> 1)) ^ (n >> 1)) << 4) + 8 * (q & 1));
    }
    f32x4 acc[4], o[4];
#pragma unroll
    for (int c = 0; c < 4; ++c) { acc[c] = 0.f; o[c] = 0.f; }
    int ns = 0;
    {
        const u32x4 rsrc = make_rsrc(x - 4096, (u32)NN * 128u + 4096u);
        if (!slow) {
            const int rot = ORD2 == 0 ? 0 : ORD2 == 1 ? ((wv + xr) & 1) : (wv + xr) % 3;
            ns = make_tab(smem, wv, lane, XROT ? (int)(blockIdx.x & 7) >> 1 : 0, rot);
            gather_group<false>(acc, rsrc, smem, wv, ns, L, make_prime(smem, wv, L));
        } else {
            slow_bucket<false>(smem, acc, bl, ovf, ovfn, bucket, rsrc, t, wv, L);
        }
    }
    STAMP(1, 3);
    Prime P2;
    if (active) {
        u32x2 tf[4];
#pragma unroll
        for (int c = 0; c < 4; ++c) {
            tf[c][0] = pack_bf16(acc[c][0], acc[c][1]);
            tf[c][1] = pack_bf16(acc[c][2], acc[c][3]);
            acc[c] = 0.f;
        }
        store_tile(t1, node0, nrows, tf, ring, lane);
        while (__hip_atomic_load(lflag + 1, __ATOMIC_RELAXED, __HIP_MEMORY_SCOPE_WORKGROUP) < 3u) __builtin_amdgcn_s_sleep(1);
        asm volatile("" ::: "memory");
        dense(o, smem, 0, xf, L);
        WAIT_VM(0);
        STAMP(1, 4);
        u32 old = 0;
        if (lane == 0) old = __hip_atomic_fetch_add(lflag, 1u, __ATOMIC_RELAXED, __HIP_MEMORY_SCOPE_WORKGROUP);
        const bool last = __builtin_amdgcn_readfirstlane(old) == (u32)(GPB - 1);
        if (last) {
            if (lane < NREP) __hip_atomic_fetch_add(bar + (lane * NSH + blockIdx.x % NSH) * 64, 1u, __ATOMIC_RELAXED, __HIP_MEMORY_SCOPE_AGENT);
            if (PF3 > 0 && lane < 8)
                __hip_atomic_store(bar + (512 + xr * 8 + lane) * 64 + (((blockIdx.x & 7u) - (lane & 6u)) & 7u), 1u, __ATOMIC_RELAXED, __HIP_MEMORY_SCOPE_AGENT);
        }
        __builtin_amdgcn_sched_barrier(0);
        dense(o, smem, 1, tf, L);
        P2 = make_prime(smem, wv, L);
        asm volatile("" : "+v"(P2.off[0]), "+v"(P2.off[1]), "+v"(P2.bq[0]), "+v"(P2.bq[1]), "+v"(P2.an));
        if (last) {
            const u32* pb = bar + ((blockIdx.x / NSH) % NREP * NSH + (lane % NSH)) * 64;
            const unsigned long long t0 = __builtin_amdgcn_s_memrealtime();
            while (__builtin_amdgcn_s_memrealtime() - t0 < 20000000ull) {
                u32 c = NBK / NSH;
                if (lane < NSH) c = __hip_atomic_load(pb, __ATOMIC_RELAXED, __HIP_MEMORY_SCOPE_AGENT);
                if (__all(c == (u32)(NBK / NSH))) break;
                __builtin_amdgcn_s_sleep(POLLSLEEP);
            }
            if (ACQ) { __builtin_amdgcn_fence(__ATOMIC_ACQUIRE, "agent"); WAIT_VM(0); }
            if (lane == 0) __hip_atomic_store(lflag + 2, 1u, __ATOMIC_RELAXED, __HIP_MEMORY_SCOPE_WORKGROUP);
        }
    }
    while (__hip_atomic_load(lflag + 2, __ATOMIC_RELAXED, __HIP_MEMORY_SCOPE_WORKGROUP) == 0u) __builtin_amdgcn_s_sleep(1);
    asm volatile("" ::: "memory");
    STAMP(1, 5);
    {
        const u32x4 rsrc = make_rsrc(t1 - 4096, (u32)NN * 128u + 4096u);
        if (!slow) gather_group<true>(acc, rsrc, smem, wv, ns, L, P2);
        else slow_bucket<true>(smem, acc, bl, ovf, ovfn, bucket, rsrc, t, wv, L);
    }
    STAMP(1, 6);
    if (!active) return;
    u32x2 f[4];
#pragma unroll
    for (int c = 0; c < 4; ++c) {
        f[c][0] = pack_bf16(2.f * acc[c][0] - bf_lo(xf[c][0]), 2.f * acc[c][1] - bf_hi(xf[c][0]));
        f[c][1] = pack_bf16(2.f * acc[c][2] - bf_lo(xf[c][1]), 2.f * acc[c][3] - bf_hi(xf[c][1]));
    }
    dense(o, smem, 2, f, L);
#pragma unroll
    for (int c = 0; c < 4; ++c) {
        f[c][0] = pack_bf16(o[c][0], o[c][1]);
        f[c][1] = pack_bf16(o[c][2], o[c][3]);
    }
    store_tile(out, node0, nrows, f, ring, lane);
}

extern "C" void kernel_launch(void* const* d_in, const int* in_sizes, int n_in, void* d_out, int out_size, void* d_ws,
                              size_t ws_size, hipStream_t stream) {
    const char* x = (const char*)d_in[0];
    const int* src = (const int*)d_in[1];
    const int* dst = (const int*)d_in[2];
    const void* ew = d_in[3];
    const char* w = (const char*)d_in[4];
    char* ws = (char*)d_ws;
    k1_partition<<<NB1, 1024, 0, stream>>>((const i32x4*)src, (const i32x4*)dst, (const u32x2*)ew, ws + W_LINE,
                                           (u32x2*)(ws + W_OVF), (u32*)(ws + W_OVFN), (u32*)(ws + W_BAR), x);
    k2_cheb<<<NBK, 1024, 0, stream>>>((const u32x4*)(ws + W_LINE), (const u32x2*)(ws + W_OVF), (const u32*)(ws + W_OVFN), x,
                                      (u32*)(ws + W_BAR), ws + W_T1, w, (char*)d_out);
}
```

```cpp
#include <hip/hip_runtime.h>
#include <hip/hip_bf16.h>
#include <stdint.h>

typedef unsigned int u32;
typedef unsigned short u16;
typedef unsigned char u8;
typedef u32 u32x4 __attribute__((ext_vector_type(4)));
typedef u32 u32x2 __attribute__((ext_vector_type(2)));
typedef int i32x4 __attribute__((ext_vector_type(4)));
typedef short s16x2 __attribute__((ext_vector_type(2)));
typedef short s16x4 __attribute__((ext_vector_type(4)));
typedef __bf16 bf16x8 __attribute__((ext_vector_type(8)));
typedef __bf16 bf16x2 __attribute__((ext_vector_type(2)));
typedef float f32x4 __attribute__((ext_vector_type(4)));
#define LAS __attribute__((address_space(3)))

constexpr int NN = 50000, NE = 800000;
constexpr int NBK = 256;
constexpr int NB1 = 250, CH = 3200;
constexpr int GPB = 13;
constexpr int SLOTS = 24;
constexpr int CAP2 = 192;

#define PFA 0
#define PFPOS 1
#define DELAY 60
#define BSYNC 0
#define IDLED 30
#define DRAIN 0
#define RETRYSLEEP 2
#define ORD2 1
#define NSH 8
#define NREP 32
#define POLLSLEEP 2

constexpr int L_LO = 0;
constexpr int L_NUL = L_LO + GPB * 2 * CAP2 * 4;
constexpr int L_DUMP = L_NUL + 32;
constexpr int L_OH = L_DUMP + 32;
constexpr int L_CNT = L_OH + L_OH / 2;
constexpr int L_JUNK = L_CNT + 256;
constexpr int L_TAB = L_JUNK + 256;
constexpr int L_W = (L_TAB + GPB * 128 + 255) / 256 * 256;
constexpr int L_RING = L_W + 24576;
constexpr int RINGH = GPB * 4096;
constexpr int L_K2 = L_RING + 2 * RINGH;
constexpr int L_STAGE = L_RING;
constexpr int L_CNTA = L_STAGE + 32768;
static_assert(L_K2 <= 163840 && 32768 + 1040 <= RINGH, "lds");
static_assert(L_OH % 32 == 0 && L_CNT % 16 == 0 && L_TAB % 16 == 0, "align");
enum { F_ARR2 = 60, F_W = 61, F_GO2 = 62 };

constexpr size_t W_LINE = 0;
constexpr size_t W_OVF = W_LINE + (size_t)NBK * NB1 * 128;
constexpr size_t W_HDR = W_OVF + (size_t)NB1 * CH * 16;
constexpr size_t W_BAR = W_HDR + 2048;
constexpr size_t W_T1 = W_BAR + 512 * 256;
static_assert(W_OVF % 128 == 0 && W_BAR % 256 == 0 && W_T1 % 128 == 0 && NREP * NSH == 256, "align");
constexpr u32 HKEY = 0x9e3779b9u;
constexpr unsigned long long TMO = 20000000ull;

__device__ __forceinline__ u32 lds_addr(const void* p) { return (u32)(uintptr_t)(const LAS char*)p; }
__device__ __forceinline__ u32 bstart(u32 b) { return (b * 3125u) >> 4; }
__device__ __forceinline__ u32 bucket_of(u32 d) { return __umulhi(d * 16u + 15u, 1374390u); }

__device__ __forceinline__ u32x4 make_rsrc(const void* base, u32 bytes) {
    const uint64_t a = (uint64_t)base;
    u32x4 r;
    r[0] = (u32)a;
    r[1] = (u32)(a >> 32) & 0xffffu;
    r[2] = bytes;
    r[3] = 0x00020000u;
    return r;
}

template <bool SC1>
__device__ __forceinline__ void dma4(u32x4 rsrc, u32 lds, u32 o0, u32 o1, u32 o2, u32 o3) {
    u32 keep;
    if (SC1)
        asm volatile(
            "s_waitcnt lgkmcnt(0)\n\t"
            "s_mov_b32 %0, m0\n\t"
            "s_mov_b32 m0, %1\n\t"
            "s_nop 0\n\t"
            "buffer_load_dwordx4 %2, %6, 0 offen sc1 lds\n\t"
            "buffer_load_dwordx4 %3, %6, 0 offen offset:1024 sc1 lds\n\t"
            "buffer_load_dwordx4 %4, %6, 0 offen offset:2048 sc1 lds\n\t"
            "buffer_load_dwordx4 %5, %6, 0 offen offset:3072 sc1 lds\n\t"
            "s_mov_b32 m0, %0"
            : "=&s"(keep)
            : "s"(lds), "v"(o0), "v"(o1), "v"(o2), "v"(o3), "s"(rsrc)
            : "memory");
    else
        asm volatile(
            "s_waitcnt lgkmcnt(0)\n\t"
            "s_mov_b32 %0, m0\n\t"
            "s_mov_b32 m0, %1\n\t"
            "s_nop 0\n\t"
            "buffer_load_dwordx4 %2, %6, 0 offen lds\n\t"
            "buffer_load_dwordx4 %3, %6, 0 offen offset:1024 lds\n\t"
            "buffer_load_dwordx4 %4, %6, 0 offen offset:2048 lds\n\t"
            "buffer_load_dwordx4 %5, %6, 0 offen offset:3072 lds\n\t"
            "s_mov_b32 m0, %0"
            : "=&s"(keep)
            : "s"(lds), "v"(o0), "v"(o1), "v"(o2), "v"(o3), "s"(rsrc)
            : "memory");
}
__device__ __forceinline__ void dma2(u32x4 rsrc, u32 lds, u32 o0, u32 o1) {
    u32 keep;
    asm volatile(
        "s_waitcnt lgkmcnt(0)\n\t"
        "s_mov_b32 %0, m0\n\t"
        "s_mov_b32 m0, %1\n\t"
        "s_nop 0\n\t"
        "buffer_load_dwordx4 %2, %4, 0 offen lds\n\t"
        "buffer_load_dwordx4 %3, %4, 0 offen offset:1024 lds\n\t"
        "s_mov_b32 m0, %0"
        : "=&s"(keep)
        : "s"(lds), "v"(o0), "v"(o1), "s"(rsrc)
        : "memory");
}
__device__ __forceinline__ void touch(u32x4 rsrc, u32 junk_lds, u32 off) {
    u32 keep;
    asm volatile(
        "s_mov_b32 %0, m0\n\t"
        "s_mov_b32 m0, %1\n\t"
        "s_nop 0\n\t"
        "buffer_load_dword %2, %3, 0 offen sc1 lds\n\t"
        "s_mov_b32 m0, %0"
        : "=&s"(keep)
        : "s"(junk_lds), "v"(off), "s"(rsrc)
        : "memory");
}
#define WAIT_VM(n) asm volatile("s_waitcnt vmcnt(" #n ")" ::: "memory")
#define RLX_AGENT __ATOMIC_RELAXED, __HIP_MEMORY_SCOPE_AGENT
#define RLX_WG __ATOMIC_RELAXED, __HIP_MEMORY_SCOPE_WORKGROUP
__device__ __forceinline__ u32x4 ld16(const void* base, u32 bytes, u32 off) {
    return __builtin_amdgcn_raw_buffer_load_b128(__builtin_amdgcn_make_buffer_rsrc((void*)base, 0, bytes, 0x00020000), off, 0, 16);
}
__device__ __forceinline__ void st16(void* p, u32x4 v) {
    asm volatile("global_store_dwordx4 %0, %1, off sc1\n\ts_nop 1" ::"v"(p), "v"(v) : "memory");
}

#define STAMP_DECL do {} while (0)
#define STAMP(k, i) do {} while (0)
#define STAMP_FLUSH(k) do {} while (0)

__device__ __forceinline__ u32 pack_bf16(float a, float b) {
    bf16x2 v;
    v[0] = (__bf16)a;
    v[1] = (__bf16)b;
    return __builtin_bit_cast(u32, v);
}
__device__ __forceinline__ float bf_lo(u32 v) { return __uint_as_float(v << 16); }
__device__ __forceinline__ float bf_hi(u32 v) { return __uint_as_float(v & 0xffff0000u); }

struct Lane {
    u32 coff[4];
    u32 tra[4];
    u32 sh;
    int lane;
};
__device__ __forceinline__ Lane make_lane(int lane) {
    Lane L;
    L.lane = lane;
    const int a = lane >> 3, p = lane & 7;
#pragma unroll
    for (int i = 0; i < 4; ++i) L.coff[i] = (u32)(((p ^ ((((a >> 1) & 1) << 1) | ((i & 1) << 2))) << 4) + 4096 - 1024 * i);
    const int g = lane >> 4, q = (lane & 15) >> 2, pp = lane & 3;
    const int sw = ((g & 1) << 1) | ((q & 1) << 2);
#pragma unroll
    for (int c = 0; c < 4; ++c)
        L.tra[c] = (u32)(128 * (8 * (q & 1) + 2 * g + (q >> 1)) + 16 * ((2 * c + (pp >> 1)) ^ sw) + 8 * (pp & 1));
    L.sh = (u32)(15 - (lane & 15)) * 0x10001u;
    return L;
}
__device__ __forceinline__ bf16x8 afrag(const char* buf, const Lane& L, int c) {
    const s16x4 a0 = __builtin_amdgcn_ds_read_tr16_b64_v4i16((LAS s16x4*)(buf + L.tra[c]));
    const s16x4 a1 = __builtin_amdgcn_ds_read_tr16_b64_v4i16((LAS s16x4*)(buf + L.tra[c] + 2048));
    return __builtin_bit_cast(bf16x8, __builtin_shufflevector(a0, a1, 0, 1, 2, 3, 4, 5, 6, 7));
}
__device__ __forceinline__ u32 row_off(u32 lo, u32 add) {
    u32 r;
    asm("v_mad_u32_u16 %0, %1, %2, %3" : "=v"(r) : "v"(lo), "s"(128u), "v"(add));
    return r;
}
__device__ __forceinline__ u32 sel2(u32 oh, u32 lo0, u32 lo1, u32 sh) {
    const s16x2 m = (__builtin_bit_cast(s16x2, oh) << __builtin_bit_cast(s16x2, sh)) >> 15;
    return __builtin_amdgcn_perm(lo1, lo0, 0x07060302u) & __builtin_bit_cast(u32, m);
}
__device__ __forceinline__ int make_tab(char* smem, int wv, int lane, int q0, int rot) {
    const u32x4 cv = *(const u32x4*)(smem + L_CNT + 16 + wv * 16);
    const u32 c0 = __builtin_amdgcn_readfirstlane(cv[0]), c1 = __builtin_amdgcn_readfirstlane(cv[1]);
    const u32 c2 = __builtin_amdgcn_readfirstlane(cv[2]), c3 = __builtin_amdgcn_readfirstlane(cv[3]);
    const u32 b0 = (u32)wv * 48u;
    const u32 n0 = (c0 + 31u) >> 5, s1 = max((c1 + 4u) >> 5, n0);
    const u32 e2 = (c2 + 31u) >> 5, s3 = max((c3 + 4u) >> 5, e2);
    u32 sa = b0, sb = s1, sc = b0 + 24u, sd = s3, la = n0 - b0, lb = b0 + 24u - s1, lc = e2 - sc, ld = b0 + 48u - s3, u;
    if (q0 & 1) { u = sa; sa = sb; sb = sc; sc = sd; sd = u; u = la; la = lb; lb = lc; lc = ld; ld = u; }
    if (q0 & 2) { u = sa; sa = sc; sc = u; u = sb; sb = sd; sd = u; u = la; la = lc; lc = u; u = lb; lb = ld; ld = u; }
    if (rot >= 1) { u = sa; sa = sb; sb = u; u = la; la = lb; lb = u; }
    if (rot >= 2) { u = sb; sb = sc; sc = u; u = lb; lb = lc; lc = u; }
    if (rot >= 3) { u = sc; sc = sd; sd = u; u = lc; lc = ld; ld = u; }
    const u32 ea = la, eb = ea + lb, ec = eb + lc, nb = ec + ld;
    const u32 v = (u32)lane;
    u32 pb = v < ec ? sc - eb : sd - ec;
    pb = v < eb ? sb - ea : pb;
    pb = v < ea ? sa : pb;
    const u32 a = v < nb ? (pb + v) * 32u : (u32)L_NUL;
    *(u16*)(smem + L_TAB + wv * 128 + lane * 2) = (u16)a;
    __builtin_amdgcn_wave_barrier();
    return (int)((nb + 3u) >> 2);
}
struct Blk { u32x4 ix, l0, l1, oh; };
__device__ __forceinline__ Blk read_blk(const char* smem, u32 a, const Lane& L) {
    Blk B;
    B.ix = *(const u32x4*)(smem + a + (L.lane & 8) * 2);
    B.l0 = *(const u32x4*)(smem + a);
    B.l1 = *(const u32x4*)(smem + a + 16);
    B.oh = *(const u32x4*)(smem + L_OH + (a >> 1));
    return B;
}
template <bool SC1>
__device__ __forceinline__ void issue_blk(u32x4 rsrc, const Blk& B, u32 buf, const Lane& L) {
    dma4<SC1>(rsrc, buf, row_off(B.ix[0], L.coff[0]), row_off(B.ix[1], L.coff[1]), row_off(B.ix[2], L.coff[2]), row_off(B.ix[3], L.coff[3]));
}
__device__ __forceinline__ u32x4 sel_blk(const Blk& B, const Lane& L) {
    u32x4 b;
    b[0] = sel2(B.oh[0], B.l0[0], B.l0[1], L.sh);
    b[1] = sel2(B.oh[2], B.l1[0], B.l1[1], L.sh);
    b[2] = sel2(B.oh[1], B.l0[2], B.l0[3], L.sh);
    b[3] = sel2(B.oh[3], B.l1[2], B.l1[3], L.sh);
    return b;
}
__device__ __forceinline__ void own_rows(u32x4 rsrc, u32 buf, int node0, int lane) {
    const u32 o = (u32)(node0 + (lane >> 3)) * 128u + (u32)(((lane & 7) ^ (lane >> 4)) << 4) + 4096u;
    dma2(rsrc, buf, o, o ^ 64u);
}
struct Prime { u32x4 off[2], bq[2]; u32 an; };
__device__ __forceinline__ Prime make_prime(const char* smem, int wv, const Lane& L) {
    const u16* tp = (const u16*)(smem + L_TAB + wv * 128) + (L.lane >> 4);
    Prime P;
#pragma unroll
    for (int h = 0; h < 2; ++h) {
        const Blk B = read_blk(smem, tp[4 * h], L);
#pragma unroll
        for (int i = 0; i < 4; ++i) P.off[h][i] = row_off(B.ix[i], L.coff[i]);
        P.bq[h] = sel_blk(B, L);
    }
    P.an = tp[8];
    return P;
}
template <bool SC1, bool OWN>
__device__ __forceinline__ void gather_group(f32x4 (&acc)[4], u32x4 rsrc, char* smem, int wv, int ns, const Lane& L, const Prime& P,
                                             int node0) {
    char* ring = smem + L_RING + wv * 4096;
    const u32 rb = lds_addr(ring);
    const u16* tp = (const u16*)(smem + L_TAB + wv * 128) + (L.lane >> 4);
    u32x4 bq[2] = {P.bq[0], P.bq[1]};
    __builtin_amdgcn_s_setprio(3);
#pragma unroll
    for (int h = 0; h < 2; ++h)
        if (h < ns) dma4<SC1>(rsrc, rb + h * RINGH, P.off[h][0], P.off[h][1], P.off[h][2], P.off[h][3]);
        else if (OWN && h == ns) own_rows(rsrc, rb + h * RINGH, node0, L.lane);
    u32 an = P.an;
    int rem = ns;
    while (rem > 0) {
#pragma unroll
        for (int h = 0; h < 2; ++h) {
            if (h == 1 && rem == 0) break;
            if (rem == 7) __builtin_amdgcn_s_setprio(2);
            else if (rem == 5) __builtin_amdgcn_s_setprio(1);
            else if (rem == 3) __builtin_amdgcn_s_setprio(0);
            const Blk B = read_blk(smem, an, L);
            an = tp[4 * (h + 3)];
            if (rem > 1) WAIT_VM(4); else if (OWN) WAIT_VM(2); else WAIT_VM(0);
            bf16x8 a[4];
#pragma unroll
            for (int c = 0; c < 4; ++c) a[c] = afrag(ring + h * RINGH, L, c);
            if (rem > 2) issue_blk<SC1>(rsrc, B, rb + h * RINGH, L);
            else if (OWN && rem == 2) own_rows(rsrc, rb + h * RINGH, node0, L.lane);
#pragma unroll
            for (int c = 0; c < 4; ++c)
                acc[c] = __builtin_amdgcn_mfma_f32_16x16x32_bf16(a[c], __builtin_bit_cast(bf16x8, bq[h]), acc[c], 0, 0, 0);
            bq[h] = sel_blk(B, L);
            --rem;
        }
        tp += 8;
    }
    __builtin_amdgcn_s_setprio(0);
    if (OWN) WAIT_VM(0);
}

__device__ __forceinline__ void put_record(char* smem, u32 lo, u32 meta) {
    const u32 q = ((lo & 0xffffu) * 1342u) >> 24;
    u32 a = atomicAdd((u32*)(smem + L_CNT + (meta & 0xf0u) + q * 4u), (q & 1u) ? (u32)-4 : 4u);
    a = min(a, (u32)L_DUMP);
    *(u32*)(smem + a) = lo;
    *(u16*)(smem + L_OH + (a >> 1)) = (u16)(1u << (meta & 15u));
}
struct Pos { u32 a[3], oh[3]; bool ok[3]; };
__device__ __forceinline__ Pos claim_chunk(char* smem, u32x4 v) {
    Pos P;
#pragma unroll
    for (int k = 0; k < 3; ++k) {
        const u32 g = (v[3] >> (8 * k)) & 0xf0u;
        const u32 m = (v[k] & 0xffffu) * 1342u;
        u32 dir;
        asm("v_bfe_i32 %0, %1, 24, 1\n\tv_lshl_or_b32 %0, %0, 3, 4" : "=v"(dir) : "v"(m));
        if (k == 0) asm("v_lshlrev_b16_sdwa %0, %1, %2 dst_sel:DWORD dst_unused:UNUSED_PAD src0_sel:BYTE_0 src1_sel:DWORD" : "=v"(P.oh[k]) : "v"(v[3]), "v"(1u));
        if (k == 1) asm("v_lshlrev_b16_sdwa %0, %1, %2 dst_sel:DWORD dst_unused:UNUSED_PAD src0_sel:BYTE_1 src1_sel:DWORD" : "=v"(P.oh[k]) : "v"(v[3]), "v"(1u));
        if (k == 2) asm("v_lshlrev_b16_sdwa %0, %1, %2 dst_sel:DWORD dst_unused:UNUSED_PAD src0_sel:BYTE_2 src1_sel:DWORD" : "=v"(P.oh[k]) : "v"(v[3]), "v"(1u));
        P.ok[k] = g != 0u;
        P.a[k] = dir;
        if (P.ok[k]) P.a[k] = atomicAdd((u32*)(smem + L_CNT + (g | ((m >> 22) & 0xcu))), dir);
    }
    return P;
}
__device__ __forceinline__ void write_chunk(char* smem, Pos& P, u32x4 v) {
    asm volatile("" : "+v"(P.a[0]), "+v"(P.a[1]), "+v"(P.a[2]));
#pragma unroll
    for (int k = 0; k < 3; ++k) {
        const u32 p = min(P.a[k], (u32)L_DUMP);
        if (P.ok[k]) {
            *(u32*)(smem + p) = v[k];
            *(u16*)(smem + L_OH + (p >> 1)) = (u16)P.oh[k];
        }
    }
}
__device__ __forceinline__ void put_surplus(char* smem, u32x4 r, u32 bucket) {
    if (bucket_of(r[1]) == bucket) put_record(smem, r[0], r[1] - bstart(bucket) + 16u);
}
__device__ __forceinline__ u32 wait_hdr(const unsigned long long* hdr, int b, u32 epoch) {
    const unsigned long long t0 = __builtin_amdgcn_s_memrealtime();
    for (;;) {
        const unsigned long long h = __hip_atomic_load(hdr + b, RLX_AGENT);
        if (__builtin_amdgcn_readfirstlane((u32)(h >> 32)) == (epoch ^ HKEY)) return min((u32)__builtin_amdgcn_readfirstlane((u32)h), (u32)CH);
        if (__builtin_amdgcn_s_memrealtime() - t0 > TMO) return 0u;
        __builtin_amdgcn_s_sleep(2);
    }
}
template <bool FIRST>
__device__ __forceinline__ void init_lists(char* smem, int t) {
#pragma unroll
    for (int h = 0; h < 2; ++h) {
        const int i = t + h * 1024;
        if (i < L_CNT / 16) *(u32x4*)(smem + i * 16) = (u32x4)(i < L_OH / 16 ? 0xffffffffu : 0u);
    }
    if (t >= 960) {
        const u32 b = (u32)t - 960u - 4u;
        if (b < 4u * GPB) *(u32*)(smem + L_CNT + 16 + b * 4) = (b >> 1) * (CAP2 * 4u) + ((b & 1u) ? (CAP2 - 1) * 4u : 0u);
        else if (FIRST) *(u32*)(smem + L_CNT + 16 + (int)b * 4) = 0u;
    }
}

template <bool SC1>
__device__ __forceinline__ void slow_bucket(char* smem, f32x4 (&acc)[4], const char* bl, const char* ovf,
                                            const unsigned long long* hdr, u32 epoch, u32 bucket,
                                            u32x4 rsrc, int t, int wv, const Lane& L) {
    int blk = -((NB1 + 7) / 8);
    u32 o0 = 0, n = 0;
    for (;;) {
        if (blk >= 0 && o0 >= n) {
            for (; blk < NB1; ++blk)
                if (__any(__hip_atomic_load((const u32*)(bl + blk * 128 + (t & 7) * 16 + 12), RLX_AGENT) >> 31)) break;
            if (blk >= NB1) break;
            n = wait_hdr(hdr, blk, epoch);
            o0 = 0;
        }
        __syncthreads();
        init_lists<false>(smem, t);
        __syncthreads();
        if (blk < 0) {
            const int c = (blk + (NB1 + 7) / 8) * 64 + t;
            if (t < 64 && c < NB1 * 8) { const u32x4 v = ld16(bl, NB1 * 128, c * 16); Pos P = claim_chunk(smem, v); write_chunk(smem, P, v); }
            ++blk;
        } else {
            if (t < CAP2 && o0 + t < n) put_surplus(smem, ld16(ovf, NB1 * CH * 16, (blk * CH + o0 + t) * 16), bucket);
            o0 += CAP2;
            if (o0 >= n) ++blk;
        }
        __syncthreads();
        if (wv < GPB) {
            const int ns = make_tab(smem, wv, L.lane, 0, 0);
            gather_group<SC1, false>(acc, rsrc, smem, wv, ns, L, make_prime(smem, wv, L), 0);
        }
    }
}

__device__ __forceinline__ void store_tile(char* __restrict__ outp, int node0, int nrows, const u32x2 (&v)[4], char* buf, int lane) {
    const int n = lane & 15, q = lane >> 4;
#pragma unroll
    for (int c = 0; c < 4; ++c) *(u32x2*)(buf + n * 128 + (((2 * c + (q >> 1)) ^ (n & 7)) << 4) + 8 * (q & 1)) = v[c];
    __builtin_amdgcn_wave_barrier();
    char* p = outp + (size_t)node0 * 128 + lane * 16;
    const int r = lane >> 3;
#pragma unroll
    for (int h = 0; h < 2; ++h)
        if (r + 8 * h < nrows) st16(p + h * 1024, *(const u32x4*)(buf + h * 1024 + r * 128 + (((lane & 7) ^ r) << 4)));
}
__device__ __forceinline__ void dense(f32x4 (&o)[4], const char* smem, int k, const u32x2 (&f)[4], const Lane& L) {
#pragma unroll
    for (int kk = 0; kk < 2; ++kk) {
        u32x4 b;
        b[0] = f[2 * kk][0]; b[1] = f[2 * kk][1]; b[2] = f[2 * kk + 1][0]; b[3] = f[2 * kk + 1][1];
#pragma unroll
        for (int c = 0; c < 4; ++c)
            o[c] = __builtin_amdgcn_mfma_f32_16x16x32_bf16(afrag(smem + L_W + (2 * k + kk) * 4096, L, c), __builtin_bit_cast(bf16x8, b), o[c], 0, 0, 0);
    }
}

__device__ __forceinline__ void bar_arrive(u32* bar, int lane) {
    if (lane < NREP) __hip_atomic_fetch_add(bar + (lane * NSH + blockIdx.x % NSH) * 64, 1u, RLX_AGENT);
}
__device__ __forceinline__ void bar_wait(const u32* bar, int lane) {
    const u32* pb = bar + ((blockIdx.x / NSH) % NREP * NSH + (lane % NSH)) * 64;
    const unsigned long long t0 = __builtin_amdgcn_s_memrealtime();
    while (__builtin_amdgcn_s_memrealtime() - t0 < TMO) {
        u32 c = NBK / NSH;
        if (lane < NSH) c = __hip_atomic_load(pb, RLX_AGENT);
        if (__all(c == (u32)(NBK / NSH))) break;
        __builtin_amdgcn_s_sleep(POLLSLEEP);
    }
}
__device__ __forceinline__ void lds_wait(const u32* f, u32 n) {
    while (__hip_atomic_load(f, RLX_WG) < n) __builtin_amdgcn_s_sleep(1);
    asm volatile("" ::: "memory");
}

__device__ __forceinline__ void wait_lines(u32x4& v, const char* bl, u32 off, u32 tag, int lane) {
    const bool in = off < (u32)(NB1 * 128);
    const unsigned long long t0 = __builtin_amdgcn_s_memrealtime();
    for (;;) {
        unsigned long long bad = __ballot(in && ((v[3] >> 24) & 0x7fu) != tag);
        if (bad == 0ull) break;
        if (__builtin_amdgcn_s_memrealtime() - t0 > TMO) { v[3] = 0u; break; }
        __builtin_amdgcn_s_sleep(RETRYSLEEP);
        asm volatile("" ::: "memory");
        if ((bad >> (lane & 56)) & 0xffull) v = ld16(bl, NB1 * 128, off);
    }
}
__device__ __forceinline__ void take_surplus(char* smem, unsigned long long f, int b0, const char* ovf, const unsigned long long* hdr,
                                             u32 epoch, u32 bucket, int lane) {
    while (f) {
        const int j = __builtin_ctzll(f) >> 3, b = b0 + j;
        const u32 m = (u32)(f >> (8 * j)) & 0xffu;
        f &= ~(0xffull << (8 * j));
        bool done = false;
        for (int it = 0; it < 4 && m < 64u; ++it) {
            asm volatile("" ::: "memory");
            const u32x4 r = ld16(ovf, NB1 * CH * 16, (u32)(b * CH + lane) * 16u);
            const bool mine = r[2] == epoch && r[3] == ~epoch && bucket_of(r[1] & 0xffffu) == bucket;
            if ((u32)__builtin_popcountll(__ballot(mine)) == m) {
                if (mine) put_surplus(smem, r, bucket);
                done = true;
                break;
            }
        }
        if (!done) {
            const u32 n = wait_hdr(hdr, b, epoch);
            for (u32 o = lane; o < n; o += 64) put_surplus(smem, ld16(ovf, NB1 * CH * 16, (b * CH + o) * 16u), bucket);
        }
    }
}

__global__ __launch_bounds__(1024) void k_cheb(const i32x4* __restrict__ src4, const i32x4* __restrict__ dst4,
                                               const u32x2* __restrict__ ew4, const char* __restrict__ x,
                                               const char* __restrict__ w, char* ws, char* __restrict__ out) {
    __shared__ __attribute__((aligned(256))) char smem[L_K2];
    char* lines = ws + W_LINE;
    char* ovf = ws + W_OVF;
    unsigned long long* hdr = (unsigned long long*)(ws + W_HDR);
    u32* bar = (u32*)(ws + W_BAR);
    char* t1 = ws + W_T1;
    const int t = threadIdx.x, lane = t & 63, wv = __builtin_amdgcn_readfirstlane(t >> 6);
    const int blk = blockIdx.x;
    const int xr = blk >> 3;
    const int bucket = (blk & 7) * 32 + xr;
    const bool active = wv < GPB;
    const int node0 = (int)bstart(bucket) + wv * 16;
    const int nrows = min((int)bstart(bucket + 1) - node0, 16);
    char* ring = smem + L_RING + wv * 4096;
    u32* lflag = (u32*)(smem + L_CNT);
    const u32x4 xrs = make_rsrc(x, (u32)NN * 128u);
    Lane L = make_lane(lane);
    STAMP_DECL;
    STAMP(0, 0);
    u32 epoch;
    {
        u32* stage = (u32*)(smem + L_STAGE);
        u32* cnt = (u32*)(smem + L_CNTA);
        const int e4 = min(blk, NB1 - 1) * (CH / 4) + min(t, CH / 4 - 1);
        const i32x4 d = dst4[e4];
        const u32 e0 = __hip_atomic_load(bar + 288 * 64, RLX_AGENT);
        const i32x4 s = src4[e4];
        const u32x2 ew = ew4[e4];
        if (blk == NBK - 1 && t < 256) __hip_atomic_store(bar + t * 64, 0u, RLX_AGENT);
        if (t < 512) *(u32x4*)&stage[3 * 2048 + t * 4] = (u32x4)0u;
        else if (t < 512 + 65) *(u32x4*)&cnt[(t - 512) * 4] = (u32x4)0u;
        init_lists<true>(smem, t);
        asm volatile("" : "+v"(L.coff[0]), "+v"(L.coff[1]), "+v"(L.coff[2]), "+v"(L.coff[3]), "+v"(L.tra[0]), "+v"(L.tra[1]),
                     "+v"(L.tra[2]), "+v"(L.tra[3]), "+v"(L.sh));
        __syncthreads();
        __builtin_amdgcn_sched_barrier(0);
        const bool bin = t < CH / 4 && blk < NB1;
        u32 rk[4], bk[4];
#pragma unroll
        for (int i = 0; i < 4; ++i) {
            bk[i] = bucket_of((u32)d[i]);
            rk[i] = 0u;
            if (bin) rk[i] = atomicAdd(&cnt[bk[i]], 1u);
        }
        asm volatile("" :: "v"(s), "v"(ew));
        epoch = __builtin_amdgcn_readfirstlane(e0) + 1u;
        if (bin) {
#pragma unroll
            for (int i = 0; i < 4; ++i) {
                const u32 lo = (u32)s[i] | ((i & 1) ? (ew[i >> 1] & 0xffff0000u) : (ew[i >> 1] << 16));
                const u32 c = bk[i] * 8u + (rk[i] & 7u), sub = rk[i] >> 3;
                if (__builtin_expect(rk[i] < (u32)SLOTS, 1)) {
                    stage[sub * 2048u + c] = lo;
                    ((u8*)&stage[3 * 2048 + c])[sub] = (u8)((u32)d[i] - bstart(bk[i]) + 16u);
                } else {
                    const u32 o = atomicAdd(&cnt[256], 1u);
                    u32x4 r;
                    r[0] = lo; r[1] = (u32)d[i]; r[2] = epoch; r[3] = ~epoch;
                    st16(ovf + (size_t)(blk * CH + o) * 16, r);
                }
            }
        }
        if (blk == NBK - 1) WAIT_VM(0);
        STAMP(0, 1);
        __syncthreads();
        if (blk < NB1) {
#pragma unroll
            for (int h = 0; h < 2; ++h) {
                const int c = t + h * 1024;
                const u32 m = min(max(cnt[c >> 3], (u32)SLOTS) - (u32)SLOTS, 255u);
                u32x4 v;
                v[0] = stage[c]; v[1] = stage[2048 + c]; v[2] = stage[4096 + c];
                v[3] = stage[6144 + c] | ((epoch & 0x7fu) << 24) | ((m >> (c & 7)) << 31);
                st16(lines + ((size_t)(c >> 3) * NB1 + blk) * 128 + (c & 7) * 16, v);
            }
        } else if (blk == NBK - 1 && t < 32) {
            __hip_atomic_store((unsigned long long*)(bar + (256 + t) * 64), (unsigned long long)epoch | ((unsigned long long)(epoch ^ HKEY) << 32), RLX_AGENT);
        }
        if (DRAIN) WAIT_VM(0);
    }
    STAMP(0, 2);
    const char* bl = lines + (size_t)bucket * (NB1 * 128);
    {
        auto warm = [&]() {
            if (wv >= 16 - 3 * PFA) {
                const int i = 15 - wv;
                touch(xrs, lds_addr(smem + L_JUNK), (bstart(bucket ^ (32 * (i / 3))) + (u32)((i % 3) * 64 + lane)) * 128u);
            }
        };
        if (PFPOS == 2) warm();
        if (PFPOS == 3) { __builtin_amdgcn_s_sleep(20); warm(); }
        if (DELAY) __builtin_amdgcn_s_sleep(DELAY);
        if (IDLED && blk >= NB1) __builtin_amdgcn_s_sleep(IDLED);
        u32x4 v0 = ld16(bl, NB1 * 128, t * 16);
        u32x4 v1 = ld16(bl, NB1 * 128, (t + 1024) * 16);
        if (PFPOS == 0) warm();
        wait_lines(v0, bl, t * 16, epoch & 0x7fu, lane);
        {
            Pos P0 = claim_chunk(smem, v0);
            write_chunk(smem, P0, v0);
        }
        __builtin_amdgcn_sched_barrier(0);
        wait_lines(v1, bl, (t + 1024) * 16, epoch & 0x7fu, lane);
        STAMP(0, 3);
        {
            u32* cnt = (u32*)(smem + L_CNTA);
            const u32 novf = cnt[256];
            if (__builtin_expect(novf != 0u, 0)) {
                WAIT_VM(0);
                u32 old = 0;
                if (lane == 0) old = atomicAdd(&cnt[257], 1u);
                if (old == 15u) __hip_atomic_store(hdr + blk, (unsigned long long)novf | ((unsigned long long)(epoch ^ HKEY) << 32), RLX_AGENT);
            }
        }
        if (PFPOS == 1) warm();
        {
            Pos P1 = claim_chunk(smem, v1);
            write_chunk(smem, P1, v1);
        }
        const unsigned long long f0 = __ballot(v0[3] >> 31), f1 = __ballot(v1[3] >> 31);
        if (__builtin_expect((f0 | f1) != 0ull, 0)) {
            take_surplus(smem, f0, wv * 8, ovf, hdr, epoch, bucket, lane);
            take_surplus(smem, f1, wv * 8 + 128, ovf, hdr, epoch, bucket, lane);
        }
    }
    __syncthreads();
    STAMP(0, 4);
    if (!active) {
        __builtin_amdgcn_s_setprio(3);
        const u32x4 wrs = make_rsrc(w - 4096, 3u * 64u * 128u + 4096u);
        const int a = lane >> 3;
#pragma unroll
        for (int h = 0; h < 2; ++h) {
            const int ks = (wv - GPB) * 2 + h;
            const u32 rb = (u32)(32 * ks + 4 * (a >> 1) + 2 * (a & 1)) * 128u;
            dma4<false>(wrs, lds_addr(smem + L_W + ks * 4096), rb + L.coff[0], rb + 128u + L.coff[1], rb + 2048u + L.coff[2], rb + 2176u + L.coff[3]);
        }
    }
    bool slow;
    {
        const u32x2 c = *(const u32x2*)(smem + L_CNT + 16 + min(lane, 2 * GPB - 1) * 8);
        slow = __builtin_expect(__any((int)c[0] > (int)c[1] + 4), 0);
    }
    if (!active) WAIT_VM(0);
    if (wv == 15) {
        const unsigned long long* rp = (const unsigned long long*)(bar + (256 + xr) * 64);
        const unsigned long long t0 = __builtin_amdgcn_s_memrealtime();
        while (__hip_atomic_load(rp, RLX_AGENT) != ((unsigned long long)epoch | ((unsigned long long)(epoch ^ HKEY) << 32)) &&
               __builtin_amdgcn_s_memrealtime() - t0 < TMO) __builtin_amdgcn_s_sleep(4);
    }
    if (!active && lane == 0) __hip_atomic_fetch_add(lflag + F_W, 1u, RLX_WG);
    if (!slow && !active) return;
    f32x4 acc[4], o[4];
#pragma unroll
    for (int c = 0; c < 4; ++c) { acc[c] = 0.f; o[c] = 0.f; }
    int ns = 0;
    {
        const u32x4 rsrc = make_rsrc(x - 4096, (u32)NN * 128u + 4096u);
        if (__builtin_expect(!slow, 1)) {
            ns = make_tab(smem, wv, lane, (blk & 7) >> 1, ORD2 ? ((wv + xr) & 1) : 0);
            gather_group<false, true>(acc, rsrc, smem, wv, ns, L, make_prime(smem, wv, L), node0);
        } else {
            slow_bucket<false>(smem, acc, bl, ovf, hdr, epoch, bucket, rsrc, t, wv, L);
            if (active) own_rows(rsrc, lds_addr(ring), node0, lane);
            WAIT_VM(0);
        }
    }
    STAMP(0, 5);
    Prime P2;
    u32x2 xf[4];
    if (active) {
        const int n = lane & 15, q = lane >> 4;
#pragma unroll
        for (int c = 0; c < 4; ++c)
            xf[c] = *(const u32x2*)(ring + (ns & 1) * RINGH + n * 128 + (((2 * c + (q >> 1)) ^ (n >> 1)) << 4) + 8 * (q & 1));
        u32x2 tf[4];
#pragma unroll
        for (int c = 0; c < 4; ++c) {
            tf[c][0] = pack_bf16(acc[c][0], acc[c][1]);
            tf[c][1] = pack_bf16(acc[c][2], acc[c][3]);
            acc[c] = 0.f;
        }
        store_tile(t1, node0, nrows, tf, ring + (~ns & 1) * RINGH, lane);
        lds_wait(lflag + F_W, 3u);
        dense(o, smem, 0, xf, L);
        WAIT_VM(0);
        u32 old = 0;
        if (lane == 0) old = __hip_atomic_fetch_add(lflag + F_ARR2, 1u, RLX_WG);
        const bool last = __builtin_amdgcn_readfirstlane(old) == (u32)(GPB - 1);
        if (last) bar_arrive(bar, lane);
        __builtin_amdgcn_sched_barrier(0);
        dense(o, smem, 1, tf, L);
        P2 = make_prime(smem, wv, L);
        asm volatile("" : "+v"(P2.off[0]), "+v"(P2.off[1]), "+v"(P2.bq[0]), "+v"(P2.bq[1]), "+v"(P2.an));
        if (last) {
            bar_wait(bar, lane);
            if (lane == 0) __hip_atomic_store(lflag + F_GO2, 1u, RLX_WG);
            if (blk == NBK - 1 && lane == 0) __hip_atomic_store(bar + 288 * 64, epoch, RLX_AGENT);
        }
    }
    lds_wait(lflag + F_GO2, 1u);
    STAMP(0, 6);
    {
        const u32x4 rsrc = make_rsrc(t1 - 4096, (u32)NN * 128u + 4096u);
        if (__builtin_expect(!slow, 1)) gather_group<true, false>(acc, rsrc, smem, wv, ns, L, P2, 0);
        else slow_bucket<true>(smem, acc, bl, ovf, hdr, epoch, bucket, rsrc, t, wv, L);
    }
    if (!active) return;
    u32x2 f[4];
#pragma unroll
    for (int c = 0; c < 4; ++c) {
        f[c][0] = pack_bf16(2.f * acc[c][0] - bf_lo(xf[c][0]), 2.f * acc[c][1] - bf_hi(xf[c][0]));
        f[c][1] = pack_bf16(2.f * acc[c][2] - bf_lo(xf[c][1]), 2.f * acc[c][3] - bf_hi(xf[c][1]));
    }
    dense(o, smem, 2, f, L);
#pragma unroll
    for (int c = 0; c < 4; ++c) {
        f[c][0] = pack_bf16(o[c][0], o[c][1]);
        f[c][1] = pack_bf16(o[c][2], o[c][3]);
    }
    store_tile(out, node0, nrows, f, ring, lane);
}

extern "C" void kernel_launch(void* const* d_in, const int* in_sizes, int n_in, void* d_out, int out_size, void* d_ws,
                              size_t ws_size, hipStream_t stream) {
    k_cheb<<<NBK, 1024, 0, stream>>>((const i32x4*)d_in[1], (const i32x4*)d_in[2], (const u32x2*)d_in[3],
                                     (const char*)d_in[0], (const char*)d_in[4], (char*)d_ws, (char*)d_out);
}
```

```cpp
#include <hip/hip_runtime.h>
#include <hip/hip_bf16.h>
#include <stdint.h>

typedef unsigned int u32;
typedef unsigned short u16;
typedef unsigned char u8;
typedef u32 u32x4 __attribute__((ext_vector_type(4)));
typedef u32 u32x2 __attribute__((ext_vector_type(2)));
typedef int i32x4 __attribute__((ext_vector_type(4)));
typedef short s16x2 __attribute__((ext_vector_type(2)));
typedef short s16x4 __attribute__((ext_vector_type(4)));
typedef __bf16 bf16x8 __attribute__((ext_vector_type(8)));
typedef __bf16 bf16x2 __attribute__((ext_vector_type(2)));
typedef float f32x4 __attribute__((ext_vector_type(4)));
#define LAS __attribute__((address_space(3)))

constexpr int NN = 50000, NE = 800000;
constexpr int NBK = 256;
constexpr int NB1 = 250, CH = 3200;
constexpr int GPB = 13;
constexpr int SLOTS = 24;
constexpr int CAP2 = 192;

#define DELAY 50
#define PSL 2
#define NSHV 4
constexpr int NSH = NSHV;
constexpr int NREP = 256 / NSH;

constexpr int L_LO = 0;
constexpr int L_NUL = L_LO + GPB * 2 * CAP2 * 4;
constexpr int L_DUMP = L_NUL + 32;
constexpr int L_OH = L_DUMP + 32;
constexpr int L_CNT = L_OH + L_OH / 2;
constexpr int L_TAB = L_CNT + 256;
constexpr int L_W = (L_TAB + GPB * 128 + 255) / 256 * 256;
constexpr int L_RING = L_W + 24576;
constexpr int RINGH = GPB * 4096;
constexpr int L_K2 = L_RING + 2 * RINGH;
constexpr int L_STAGE = L_RING;
constexpr int L_CNTA = L_STAGE + 32768;
static_assert(L_K2 <= 163840 && 32768 + 1040 <= RINGH, "lds");
static_assert(L_OH % 32 == 0 && L_CNT % 16 == 0 && L_TAB % 16 == 0, "align");
enum { F_ARR2 = 60, F_W = 61, F_GO2 = 62 };

constexpr size_t W_LINE = 0;
constexpr size_t W_OVF = W_LINE + (size_t)NBK * NB1 * 128;
constexpr size_t W_HDR = W_OVF + (size_t)NB1 * CH * 16;
constexpr size_t W_BAR = W_HDR + 2048;
constexpr size_t W_T1 = W_BAR + 512 * 256;
static_assert(W_OVF % 128 == 0 && W_BAR % 256 == 0 && W_T1 % 128 == 0 && NREP * NSH == 256, "align");
constexpr u32 HKEY = 0x9e3779b9u;
constexpr unsigned long long TMO = 20000000ull;

__device__ __forceinline__ u32 lds_addr(const void* p) { return (u32)(uintptr_t)(const LAS char*)p; }
__device__ __forceinline__ u32 bstart(u32 b) { return (b * 3125u) >> 4; }
__device__ __forceinline__ u32 bucket_of(u32 d) { return __umulhi(d * 16u + 15u, 1374390u); }

__device__ __forceinline__ u32x4 make_rsrc(const void* base, u32 bytes) {
    const uint64_t a = (uint64_t)base;
    u32x4 r;
    r[0] = (u32)a;
    r[1] = (u32)(a >> 32) & 0xffffu;
    r[2] = bytes;
    r[3] = 0x00020000u;
    return r;
}

template <bool SC1>
__device__ __forceinline__ void dma4(u32x4 rsrc, u32 lds, u32 o0, u32 o1, u32 o2, u32 o3) {
    u32 keep;
    if (SC1)
        asm volatile(
            "s_waitcnt lgkmcnt(0)\n\t"
            "s_mov_b32 %0, m0\n\t"
            "s_mov_b32 m0, %1\n\t"
            "s_nop 0\n\t"
            "buffer_load_dwordx4 %2, %6, 0 offen sc1 lds\n\t"
            "buffer_load_dwordx4 %3, %6, 0 offen offset:1024 sc1 lds\n\t"
            "buffer_load_dwordx4 %4, %6, 0 offen offset:2048 sc1 lds\n\t"
            "buffer_load_dwordx4 %5, %6, 0 offen offset:3072 sc1 lds\n\t"
            "s_mov_b32 m0, %0"
            : "=&s"(keep)
            : "s"(lds), "v"(o0), "v"(o1), "v"(o2), "v"(o3), "s"(rsrc)
            : "memory");
    else
        asm volatile(
            "s_waitcnt lgkmcnt(0)\n\t"
            "s_mov_b32 %0, m0\n\t"
            "s_mov_b32 m0, %1\n\t"
            "s_nop 0\n\t"
            "buffer_load_dwordx4 %2, %6, 0 offen lds\n\t"
            "buffer_load_dwordx4 %3, %6, 0 offen offset:1024 lds\n\t"
            "buffer_load_dwordx4 %4, %6, 0 offen offset:2048 lds\n\t"
            "buffer_load_dwordx4 %5, %6, 0 offen offset:3072 lds\n\t"
            "s_mov_b32 m0, %0"
            : "=&s"(keep)
            : "s"(lds), "v"(o0), "v"(o1), "v"(o2), "v"(o3), "s"(rsrc)
            : "memory");
}
__device__ __forceinline__ void dma2(u32x4 rsrc, u32 lds, u32 o0, u32 o1) {
    u32 keep;
    asm volatile(
        "s_waitcnt lgkmcnt(0)\n\t"
        "s_mov_b32 %0, m0\n\t"
        "s_mov_b32 m0, %1\n\t"
        "s_nop 0\n\t"
        "buffer_load_dwordx4 %2, %4, 0 offen lds\n\t"
        "buffer_load_dwordx4 %3, %4, 0 offen offset:1024 lds\n\t"
        "s_mov_b32 m0, %0"
        : "=&s"(keep)
        : "s"(lds), "v"(o0), "v"(o1), "s"(rsrc)
        : "memory");
}
#define WAIT_VM(n) asm volatile("s_waitcnt vmcnt(" #n ")" ::: "memory")
#define RLX_AGENT __ATOMIC_RELAXED, __HIP_MEMORY_SCOPE_AGENT
#define RLX_WG __ATOMIC_RELAXED, __HIP_MEMORY_SCOPE_WORKGROUP
__device__ __forceinline__ u32x4 ld16(const void* base, u32 bytes, u32 off) {
    return __builtin_amdgcn_raw_buffer_load_b128(__builtin_amdgcn_make_buffer_rsrc((void*)base, 0, bytes, 0x00020000), off, 0, 16);
}
__device__ __forceinline__ void st16(void* p, u32x4 v) {
    asm volatile("global_store_dwordx4 %0, %1, off sc1\n\ts_nop 1" ::"v"(p), "v"(v) : "memory");
}

#define STAMP_DECL do {} while (0)
#define STAMP(k, i) do {} while (0)
#define STAMP_FLUSH(k) do {} while (0)

__device__ __forceinline__ u32 pack_bf16(float a, float b) {
    bf16x2 v;
    v[0] = (__bf16)a;
    v[1] = (__bf16)b;
    return __builtin_bit_cast(u32, v);
}
__device__ __forceinline__ float bf_lo(u32 v) { return __uint_as_float(v << 16); }
__device__ __forceinline__ float bf_hi(u32 v) { return __uint_as_float(v & 0xffff0000u); }

struct Lane {
    u32 coff[4];
    u32 tra[4];
    u32 sh;
    int lane;
};
__device__ __forceinline__ Lane make_lane(int lane) {
    Lane L;
    L.lane = lane;
    const int a = lane >> 3, p = lane & 7;
#pragma unroll
    for (int i = 0; i < 4; ++i) L.coff[i] = (u32)(((p ^ ((((a >> 1) & 1) << 1) | ((i & 1) << 2))) << 4) + 4096 - 1024 * i);
    const int g = lane >> 4, q = (lane & 15) >> 2, pp = lane & 3;
    const int sw = ((g & 1) << 1) | ((q & 1) << 2);
#pragma unroll
    for (int c = 0; c < 4; ++c)
        L.tra[c] = (u32)(128 * (8 * (q & 1) + 2 * g + (q >> 1)) + 16 * ((2 * c + (pp >> 1)) ^ sw) + 8 * (pp & 1));
    L.sh = (u32)(15 - (lane & 15)) * 0x10001u;
    return L;
}
__device__ __forceinline__ bf16x8 afrag(const char* buf, const Lane& L, int c) {
    const s16x4 a0 = __builtin_amdgcn_ds_read_tr16_b64_v4i16((LAS s16x4*)(buf + L.tra[c]));
    const s16x4 a1 = __builtin_amdgcn_ds_read_tr16_b64_v4i16((LAS s16x4*)(buf + L.tra[c] + 2048));
    return __builtin_bit_cast(bf16x8, __builtin_shufflevector(a0, a1, 0, 1, 2, 3, 4, 5, 6, 7));
}
__device__ __forceinline__ u32 row_off(u32 lo, u32 add) {
    u32 r;
    asm("v_mad_u32_u16 %0, %1, %2, %3" : "=v"(r) : "v"(lo), "s"(128u), "v"(add));
    return r;
}
__device__ __forceinline__ u32 sel2(u32 oh, u32 lo0, u32 lo1, u32 sh) {
    const s16x2 m = (__builtin_bit_cast(s16x2, oh) << __builtin_bit_cast(s16x2, sh)) >> 15;
    return __builtin_amdgcn_perm(lo1, lo0, 0x07060302u) & __builtin_bit_cast(u32, m);
}
__device__ __forceinline__ int make_tab(char* smem, int wv, int lane) {
    const u32x4 cv = *(const u32x4*)(smem + L_CNT + 16 + wv * 16);
    const u32 c0 = __builtin_amdgcn_readfirstlane(cv[0]), c1 = __builtin_amdgcn_readfirstlane(cv[1]);
    const u32 c2 = __builtin_amdgcn_readfirstlane(cv[2]), c3 = __builtin_amdgcn_readfirstlane(cv[3]);
    const u32 b0 = (u32)wv * 48u;
    const u32 n0 = (c0 + 31u) >> 5, s1 = max((c1 + 4u) >> 5, n0);
    const u32 e2 = (c2 + 31u) >> 5, s3 = max((c3 + 4u) >> 5, e2);
    const u32 ea = n0 - b0, eb = ea + (e2 - s1), nb = eb + (b0 + 48u - s3), v = (u32)lane;
    u32 pb = v < eb ? s1 - ea : s3 - eb;
    pb = v < ea ? b0 : pb;
    const u32 a = v < nb ? (pb + v) * 32u : (u32)L_NUL;
    *(u16*)(smem + L_TAB + wv * 128 + lane * 2) = (u16)a;
    __builtin_amdgcn_wave_barrier();
    return (int)((nb + 3u) >> 2);
}
struct Blk { u32x4 ix, l0, l1, oh; };
__device__ __forceinline__ Blk read_blk(const char* smem, u32 a, const Lane& L) {
    Blk B;
    B.ix = *(const u32x4*)(smem + a + (L.lane & 8) * 2);
    B.l0 = *(const u32x4*)(smem + a);
    B.l1 = *(const u32x4*)(smem + a + 16);
    B.oh = *(const u32x4*)(smem + L_OH + (a >> 1));
    return B;
}
template <bool SC1>
__device__ __forceinline__ void issue_blk(u32x4 rsrc, const Blk& B, u32 buf, const Lane& L) {
    dma4<SC1>(rsrc, buf, row_off(B.ix[0], L.coff[0]), row_off(B.ix[1], L.coff[1]), row_off(B.ix[2], L.coff[2]), row_off(B.ix[3], L.coff[3]));
}
__device__ __forceinline__ u32x4 sel_blk(const Blk& B, const Lane& L) {
    u32x4 b;
    b[0] = sel2(B.oh[0], B.l0[0], B.l0[1], L.sh);
    b[1] = sel2(B.oh[2], B.l1[0], B.l1[1], L.sh);
    b[2] = sel2(B.oh[1], B.l0[2], B.l0[3], L.sh);
    b[3] = sel2(B.oh[3], B.l1[2], B.l1[3], L.sh);
    return b;
}
__device__ __forceinline__ void own_rows(u32x4 rsrc, u32 buf, int node0, int lane) {
    const u32 o = (u32)(node0 + (lane >> 3)) * 128u + (u32)(((lane & 7) ^ (lane >> 4)) << 4) + 4096u;
    dma2(rsrc, buf, o, o ^ 64u);
}
struct Prime { u32x4 off[2], bq[2]; u32 an; };
__device__ __forceinline__ Prime make_prime(const char* smem, int wv, const Lane& L) {
    const u16* tp = (const u16*)(smem + L_TAB + wv * 128) + (L.lane >> 4);
    Prime P;
#pragma unroll
    for (int h = 0; h < 2; ++h) {
        const Blk B = read_blk(smem, tp[4 * h], L);
#pragma unroll
        for (int i = 0; i < 4; ++i) P.off[h][i] = row_off(B.ix[i], L.coff[i]);
        P.bq[h] = sel_blk(B, L);
    }
    P.an = tp[8];
    return P;
}
template <bool SC1, bool OWN>
__device__ __forceinline__ void gather_group(f32x4 (&acc)[4], u32x4 rsrc, char* smem, int wv, int ns, const Lane& L, const Prime& P,
                                             int node0) {
    char* ring = smem + L_RING + wv * 4096;
    const u32 rb = lds_addr(ring);
    const u16* tp = (const u16*)(smem + L_TAB + wv * 128) + (L.lane >> 4);
    u32x4 bq[2] = {P.bq[0], P.bq[1]};
    __builtin_amdgcn_s_setprio(3);
#pragma unroll
    for (int h = 0; h < 2; ++h)
        if (h < ns) dma4<SC1>(rsrc, rb + h * RINGH, P.off[h][0], P.off[h][1], P.off[h][2], P.off[h][3]);
        else if (OWN && h == ns) own_rows(rsrc, rb + h * RINGH, node0, L.lane);
    u32 an = P.an;
    int rem = ns;
    while (rem > 0) {
#pragma unroll
        for (int h = 0; h < 2; ++h) {
            if (h == 1 && rem == 0) break;
            if (rem == 7) __builtin_amdgcn_s_setprio(2);
            else if (rem == 5) __builtin_amdgcn_s_setprio(1);
            else if (rem == 3) __builtin_amdgcn_s_setprio(0);
            const Blk B = read_blk(smem, an, L);
            an = tp[4 * (h + 3)];
            if (rem > 1) WAIT_VM(4); else if (OWN) WAIT_VM(2); else WAIT_VM(0);
            bf16x8 a[4];
#pragma unroll
            for (int c = 0; c < 4; ++c) a[c] = afrag(ring + h * RINGH, L, c);
            if (rem > 2) issue_blk<SC1>(rsrc, B, rb + h * RINGH, L);
            else if (OWN && rem == 2) own_rows(rsrc, rb + h * RINGH, node0, L.lane);
#pragma unroll
            for (int c = 0; c < 4; ++c)
                acc[c] = __builtin_amdgcn_mfma_f32_16x16x32_bf16(a[c], __builtin_bit_cast(bf16x8, bq[h]), acc[c], 0, 0, 0);
            bq[h] = sel_blk(B, L);
            --rem;
        }
        tp += 8;
    }
    __builtin_amdgcn_s_setprio(0);
}

__device__ __forceinline__ void put_record(char* smem, u32 lo, u32 meta) {
    const u32 q = ((lo & 0xffffu) * 1342u) >> 24;
    u32 a = atomicAdd((u32*)(smem + L_CNT + (meta & 0xf0u) + q * 4u), (q & 1u) ? (u32)-4 : 4u);
    a = min(a, (u32)L_DUMP);
    *(u32*)(smem + a) = lo;
    *(u16*)(smem + L_OH + (a >> 1)) = (u16)(1u << (meta & 15u));
}
struct Pos { u32 a[3], oh[3]; bool ok[3]; };
__device__ __forceinline__ Pos claim_chunk(char* smem, u32x4 v) {
    Pos P;
#pragma unroll
    for (int k = 0; k < 3; ++k) {
        const u32 g = (v[3] >> (8 * k)) & 0xf0u;
        const u32 m = (v[k] & 0xffffu) * 1342u;
        u32 dir;
        asm("v_bfe_i32 %0, %1, 24, 1\n\tv_lshl_or_b32 %0, %0, 3, 4" : "=v"(dir) : "v"(m));
        if (k == 0) asm("v_lshlrev_b16_sdwa %0, %1, %2 dst_sel:DWORD dst_unused:UNUSED_PAD src0_sel:BYTE_0 src1_sel:DWORD" : "=v"(P.oh[k]) : "v"(v[3]), "v"(1u));
        if (k == 1) asm("v_lshlrev_b16_sdwa %0, %1, %2 dst_sel:DWORD dst_unused:UNUSED_PAD src0_sel:BYTE_1 src1_sel:DWORD" : "=v"(P.oh[k]) : "v"(v[3]), "v"(1u));
        if (k == 2) asm("v_lshlrev_b16_sdwa %0, %1, %2 dst_sel:DWORD dst_unused:UNUSED_PAD src0_sel:BYTE_2 src1_sel:DWORD" : "=v"(P.oh[k]) : "v"(v[3]), "v"(1u));
        P.ok[k] = g != 0u;
        P.a[k] = dir;
        if (P.ok[k]) P.a[k] = atomicAdd((u32*)(smem + L_CNT + (g | ((m >> 22) & 0xcu))), dir);
    }
    return P;
}
__device__ __forceinline__ void write_chunk(char* smem, Pos& P, u32x4 v) {
    asm volatile("" : "+v"(P.a[0]), "+v"(P.a[1]), "+v"(P.a[2]));
#pragma unroll
    for (int k = 0; k < 3; ++k) {
        const u32 p = min(P.a[k], (u32)L_DUMP);
        if (P.ok[k]) {
            *(u32*)(smem + p) = v[k];
            *(u16*)(smem + L_OH + (p >> 1)) = (u16)P.oh[k];
        }
    }
}
__device__ __forceinline__ void put_surplus(char* smem, u32x4 r, u32 bucket) {
    if (bucket_of(r[1]) == bucket) put_record(smem, r[0], r[1] - bstart(bucket) + 16u);
}
__device__ __forceinline__ u32 wait_hdr(const unsigned long long* hdr, int b, u32 epoch) {
    const unsigned long long t0 = __builtin_amdgcn_s_memrealtime();
    for (;;) {
        const unsigned long long h = __hip_atomic_load(hdr + b, RLX_AGENT);
        if (__builtin_amdgcn_readfirstlane((u32)(h >> 32)) == (epoch ^ HKEY)) return min((u32)__builtin_amdgcn_readfirstlane((u32)h), (u32)CH);
        if (__builtin_amdgcn_s_memrealtime() - t0 > TMO) return 0u;
        __builtin_amdgcn_s_sleep(2);
    }
}
template <bool FIRST>
__device__ __forceinline__ void init_lists(char* smem, int t) {
#pragma unroll
    for (int h = 0; h < 2; ++h) {
        const int i = t + h * 1024;
        if (i < L_CNT / 16) *(u32x4*)(smem + i * 16) = (u32x4)(i < L_OH / 16 ? 0xffffffffu : 0u);
    }
    if (t >= 960) {
        const u32 b = (u32)t - 960u - 4u;
        if (b < 4u * GPB) *(u32*)(smem + L_CNT + 16 + b * 4) = (b >> 1) * (CAP2 * 4u) + ((b & 1u) ? (CAP2 - 1) * 4u : 0u);
        else if (FIRST) *(u32*)(smem + L_CNT + 16 + (int)b * 4) = 0u;
    }
}

template <bool SC1>
__device__ __forceinline__ void slow_bucket(char* smem, f32x4 (&acc)[4], const char* bl, const char* ovf,
                                            const unsigned long long* hdr, u32 epoch, u32 bucket,
                                            u32x4 rsrc, int t, int wv, const Lane& L) {
    int blk = -((NB1 + 7) / 8);
    u32 o0 = 0, n = 0;
    for (;;) {
        if (blk >= 0 && o0 >= n) {
            for (; blk < NB1; ++blk)
                if (__any(__hip_atomic_load((const u32*)(bl + blk * 128 + (t & 7) * 16 + 12), RLX_AGENT) >> 31)) break;
            if (blk >= NB1) break;
            n = wait_hdr(hdr, blk, epoch);
            o0 = 0;
        }
        __syncthreads();
        init_lists<false>(smem, t);
        __syncthreads();
        if (blk < 0) {
            const int c = (blk + (NB1 + 7) / 8) * 64 + t;
            if (t < 64 && c < NB1 * 8) { const u32x4 v = ld16(bl, NB1 * 128, c * 16); Pos P = claim_chunk(smem, v); write_chunk(smem, P, v); }
            ++blk;
        } else {
            if (t < CAP2 && o0 + t < n) put_surplus(smem, ld16(ovf, NB1 * CH * 16, (blk * CH + o0 + t) * 16), bucket);
            o0 += CAP2;
            if (o0 >= n) ++blk;
        }
        __syncthreads();
        if (wv < GPB) {
            const int ns = make_tab(smem, wv, L.lane);
            gather_group<SC1, false>(acc, rsrc, smem, wv, ns, L, make_prime(smem, wv, L), 0);
        }
    }
}

__device__ __forceinline__ void store_tile(char* __restrict__ outp, int node0, int nrows, const u32x2 (&v)[4], char* buf, int lane) {
    const int n = lane & 15, q = lane >> 4;
#pragma unroll
    for (int c = 0; c < 4; ++c) *(u32x2*)(buf + n * 128 + (((2 * c + (q >> 1)) ^ (n & 7)) << 4) + 8 * (q & 1)) = v[c];
    __builtin_amdgcn_wave_barrier();
    char* p = outp + (size_t)node0 * 128 + lane * 16;
    const int r = lane >> 3;
#pragma unroll
    for (int h = 0; h < 2; ++h)
        if (r + 8 * h < nrows) st16(p + h * 1024, *(const u32x4*)(buf + h * 1024 + r * 128 + (((lane & 7) ^ r) << 4)));
}
__device__ __forceinline__ void dense(f32x4 (&o)[4], const char* smem, int k, const u32x2 (&f)[4], const Lane& L) {
#pragma unroll
    for (int kk = 0; kk < 2; ++kk) {
        u32x4 b;
        b[0] = f[2 * kk][0]; b[1] = f[2 * kk][1]; b[2] = f[2 * kk + 1][0]; b[3] = f[2 * kk + 1][1];
#pragma unroll
        for (int c = 0; c < 4; ++c)
            o[c] = __builtin_amdgcn_mfma_f32_16x16x32_bf16(afrag(smem + L_W + (2 * k + kk) * 4096, L, c), __builtin_bit_cast(bf16x8, b), o[c], 0, 0, 0);
    }
}

__device__ __forceinline__ void bar_arrive(u32* bar, int lane) {
    if (lane < NREP) __hip_atomic_fetch_add(bar + (lane * NSH + blockIdx.x % NSH) * 64, 1u, RLX_AGENT);
}
__device__ __forceinline__ void bar_wait(const u32* bar, int lane) {
    const u32* pb = bar + ((blockIdx.x / NSH) % NREP * NSH + (lane % NSH)) * 64;
    const unsigned long long t0 = __builtin_amdgcn_s_memrealtime();
    u32 c = lane < NSH ? 0u : (u32)(NBK / NSH);
    while (__builtin_amdgcn_s_memrealtime() - t0 < TMO) {
        if (c != (u32)(NBK / NSH)) c = __hip_atomic_load(pb, RLX_AGENT);
        if (__all(c == (u32)(NBK / NSH))) break;
        __builtin_amdgcn_s_sleep(PSL);
    }
}
__device__ __forceinline__ void lds_wait(const u32* f, u32 n) {
    while (__hip_atomic_load(f, RLX_WG) < n) __builtin_amdgcn_s_sleep(1);
    asm volatile("" ::: "memory");
}

__device__ __forceinline__ void wait_lines(u32x4& v, const char* bl, u32 off, u32 tag, int lane) {
    const bool in = off < (u32)(NB1 * 128);
    unsigned long long bad = __ballot(in && ((v[3] >> 24) & 0x7fu) != tag);
    if (bad != 0ull) {
        const unsigned long long t0 = __builtin_amdgcn_s_memrealtime();
        do {
            __builtin_amdgcn_s_sleep(2);
            asm volatile("" ::: "memory");
            if ((bad >> (lane & 56)) & 0xffull) v = ld16(bl, NB1 * 128, off);
            asm volatile("" : "+v"(v));
            bad = __ballot(in && ((v[3] >> 24) & 0x7fu) != tag);
        } while (bad != 0ull && __builtin_amdgcn_s_memrealtime() - t0 < TMO);
        if (bad != 0ull) v[3] = 0u;
    }
}
__device__ __forceinline__ u32x4 first_surplus(unsigned long long f, int b0, const char* ovf, int lane) {
    return ld16(ovf, NB1 * CH * 16, (u32)((b0 + (__builtin_ctzll(f) >> 3)) * CH + lane) * 16u);
}
__device__ __forceinline__ void take_surplus(char* smem, unsigned long long f, int b0, u32x4 r, const char* ovf,
                                             const unsigned long long* hdr, u32 epoch, u32 bucket, int lane) {
    for (;;) {
        const int j = __builtin_ctzll(f) >> 3, b = b0 + j;
        const u32 m = (u32)(f >> (8 * j)) & 0xffu;
        bool done = false;
        for (int it = 0; m < 64u; ++it) {
            const bool mine = r[2] == epoch && r[3] == ~epoch && bucket_of(r[1] & 0xffffu) == bucket;
            if ((u32)__builtin_popcountll(__ballot(mine)) == m) {
                if (mine) put_surplus(smem, r, bucket);
                done = true;
                break;
            }
            if (it == 3) break;
            asm volatile("" ::: "memory");
            r = first_surplus(f, b0, ovf, lane);
        }
        if (!done) {
            const u32 n = wait_hdr(hdr, b, epoch);
            for (u32 o = lane; o < n; o += 64) put_surplus(smem, ld16(ovf, NB1 * CH * 16, (b * CH + o) * 16u), bucket);
        }
        f &= ~(0xffull << (8 * j));
        if (f == 0ull) break;
        r = first_surplus(f, b0, ovf, lane);
    }
}

__global__ __launch_bounds__(1024) void k_cheb(const u32* __restrict__ src, const u32* __restrict__ dst,
                                               const u16* __restrict__ ewp, const char* __restrict__ x,
                                               const char* __restrict__ w, char* ws, char* __restrict__ out) {
    __shared__ __attribute__((aligned(256))) char smem[L_K2];
    char* lines = ws + W_LINE;
    char* ovf = ws + W_OVF;
    unsigned long long* hdr = (unsigned long long*)(ws + W_HDR);
    u32* bar = (u32*)(ws + W_BAR);
    char* t1 = ws + W_T1;
    const int t = threadIdx.x, lane = t & 63, wv = __builtin_amdgcn_readfirstlane(t >> 6);
    const int blk = blockIdx.x;
    const int xr = blk >> 3;
    const int bucket = (blk & 7) * 32 + xr;
    const bool active = wv < GPB;
    const int node0 = (int)bstart(bucket) + wv * 16;
    const int nrows = min((int)bstart(bucket + 1) - node0, 16);
    char* ring = smem + L_RING + wv * 4096;
    u32* lflag = (u32*)(smem + L_CNT);
    Lane L = make_lane(lane);
    STAMP_DECL;
    STAMP(0, 0);
    u32 epoch;
    {
        u32* stage = (u32*)(smem + L_STAGE);
        u32* cnt = (u32*)(smem + L_CNTA);
        const u32 nb = blk < NB1 ? (u32)CH : 0u;
        const size_t e0i = (size_t)min(blk, NB1 - 1) * CH;
        const u32x4 d = __builtin_amdgcn_raw_buffer_load_b128(__builtin_amdgcn_make_buffer_rsrc((void*)(dst + e0i), 0, nb * 4u, 0x00020000), t * 16, 0, 2);
        const u32 e0 = __hip_atomic_load(bar + 288 * 64, RLX_AGENT);
        const u32x4 s = __builtin_amdgcn_raw_buffer_load_b128(__builtin_amdgcn_make_buffer_rsrc((void*)(src + e0i), 0, nb * 4u, 0x00020000), t * 16, 0, 2);
        const u32x2 ew = __builtin_amdgcn_raw_buffer_load_b64(__builtin_amdgcn_make_buffer_rsrc((void*)(ewp + e0i), 0, nb * 2u, 0x00020000), t * 8, 0, 2);
        if (blk == NBK - 1 && t < 256) __hip_atomic_store(bar + t * 64, 0u, RLX_AGENT);
        if (t < 512) *(u32x4*)&stage[3 * 2048 + t * 4] = (u32x4)0u;
        else if (t < 512 + 65) *(u32x4*)&cnt[(t - 512) * 4] = (u32x4)0u;
        init_lists<true>(smem, t);
        asm volatile("" : "+v"(L.coff[0]), "+v"(L.coff[1]), "+v"(L.coff[2]), "+v"(L.coff[3]), "+v"(L.tra[0]), "+v"(L.tra[1]),
                     "+v"(L.tra[2]), "+v"(L.tra[3]), "+v"(L.sh));
        __syncthreads();
        __builtin_amdgcn_sched_barrier(0);
        const bool bin = t < CH / 4 && blk < NB1;
        u32 rk[4], bk[4];
#pragma unroll
        for (int i = 0; i < 4; ++i) {
            bk[i] = bucket_of((u32)d[i]);
            rk[i] = 0u;
            if (bin) rk[i] = atomicAdd(&cnt[bk[i]], 1u);
        }
        asm volatile("" :: "v"(s), "v"(ew));
        epoch = __builtin_amdgcn_readfirstlane(e0) + 1u;
        if (bin) {
#pragma unroll
            for (int i = 0; i < 4; ++i) {
                const u32 lo = (u32)s[i] | ((i & 1) ? (ew[i >> 1] & 0xffff0000u) : (ew[i >> 1] << 16));
                const u32 c = bk[i] * 8u + (rk[i] & 7u), sub = rk[i] >> 3;
                if (__builtin_expect(rk[i] < (u32)SLOTS, 1)) {
                    stage[sub * 2048u + c] = lo;
                    ((u8*)&stage[3 * 2048 + c])[sub] = (u8)((u32)d[i] - bstart(bk[i]) + 16u);
                } else {
                    const u32 o = atomicAdd(&cnt[256], 1u);
                    u32x4 r;
                    r[0] = lo; r[1] = (u32)d[i]; r[2] = epoch; r[3] = ~epoch;
                    st16(ovf + (size_t)(blk * CH + o) * 16, r);
                }
            }
        }
        if (blk == NBK - 1) WAIT_VM(0);
        STAMP(0, 1);
        __syncthreads();
        if (blk < NB1) {
#pragma unroll
            for (int h = 0; h < 2; ++h) {
                const int c = t + h * 1024;
                const u32 m = min(max(cnt[c >> 3], (u32)SLOTS) - (u32)SLOTS, 255u);
                u32x4 v;
                v[0] = stage[c]; v[1] = stage[2048 + c]; v[2] = stage[4096 + c];
                v[3] = stage[6144 + c] | ((epoch & 0x7fu) << 24) | ((m >> (c & 7)) << 31);
                st16(lines + ((size_t)(c >> 3) * NB1 + blk) * 128 + (c & 7) * 16, v);
            }
        } else if (blk == NBK - 1 && t < 32) {
            __hip_atomic_store((unsigned long long*)(bar + (256 + t) * 64), (unsigned long long)epoch | ((unsigned long long)(epoch ^ HKEY) << 32), RLX_AGENT);
        }
    }
    STAMP(0, 2);
    const char* bl = lines + (size_t)bucket * (NB1 * 128);
    {
        __builtin_amdgcn_s_sleep(DELAY);
        if (blk >= NB1) __builtin_amdgcn_s_sleep(30);
        u32x4 v0 = ld16(bl, NB1 * 128, t * 16);
        u32x4 v1 = ld16(bl, NB1 * 128, (t + 1024) * 16);
        u32x4 r0 = 0u, r1 = 0u;
        wait_lines(v0, bl, t * 16, epoch & 0x7fu, lane);
        const unsigned long long f0 = __ballot(v0[3] >> 31);
        if (__builtin_expect(f0 != 0ull, 0)) r0 = first_surplus(f0, wv * 8, ovf, lane);
        {
            Pos P0 = claim_chunk(smem, v0);
            write_chunk(smem, P0, v0);
        }
        __builtin_amdgcn_sched_barrier(0);
        wait_lines(v1, bl, (t + 1024) * 16, epoch & 0x7fu, lane);
        STAMP(0, 3);
        const unsigned long long f1 = __ballot(v1[3] >> 31);
        if (__builtin_expect(f1 != 0ull, 0)) r1 = first_surplus(f1, wv * 8 + 128, ovf, lane);
        {
            Pos P1 = claim_chunk(smem, v1);
            write_chunk(smem, P1, v1);
        }
        {
            u32* cnt = (u32*)(smem + L_CNTA);
            const u32 novf = cnt[256];
            if (__builtin_expect(novf != 0u, 0)) {
                WAIT_VM(0);
                u32 old = 0;
                if (lane == 0) old = atomicAdd(&cnt[257], 1u);
                if (old == 15u) __hip_atomic_store(hdr + blk, (unsigned long long)novf | ((unsigned long long)(epoch ^ HKEY) << 32), RLX_AGENT);
            }
        }
        if (__builtin_expect(f0 != 0ull, 0)) take_surplus(smem, f0, wv * 8, r0, ovf, hdr, epoch, bucket, lane);
        if (__builtin_expect(f1 != 0ull, 0)) take_surplus(smem, f1, wv * 8 + 128, r1, ovf, hdr, epoch, bucket, lane);
    }
    __syncthreads();
    STAMP(0, 4);
    if (!active) {
        __builtin_amdgcn_s_setprio(3);
        const u32x4 wrs = make_rsrc(w - 4096, 3u * 64u * 128u + 4096u);
        const int a = lane >> 3;
#pragma unroll
        for (int h = 0; h < 2; ++h) {
            const int ks = (wv - GPB) * 2 + h;
            const u32 rb = (u32)(32 * ks + 4 * (a >> 1) + 2 * (a & 1)) * 128u;
            dma4<false>(wrs, lds_addr(smem + L_W + ks * 4096), rb + L.coff[0], rb + 128u + L.coff[1], rb + 2048u + L.coff[2], rb + 2176u + L.coff[3]);
        }
    }
    bool slow;
    {
        const u32x2 c = *(const u32x2*)(smem + L_CNT + 16 + min(lane, 2 * GPB - 1) * 8);
        slow = __builtin_expect(__any((int)c[0] > (int)c[1] + 4), 0);
    }
    if (!active) WAIT_VM(0);
    if (wv == 15) {
        const unsigned long long* rp = (const unsigned long long*)(bar + (256 + xr) * 64);
        const unsigned long long t0 = __builtin_amdgcn_s_memrealtime();
        while (__hip_atomic_load(rp, RLX_AGENT) != ((unsigned long long)epoch | ((unsigned long long)(epoch ^ HKEY) << 32)) &&
               __builtin_amdgcn_s_memrealtime() - t0 < TMO) __builtin_amdgcn_s_sleep(4);
    }
    if (!active && lane == 0) __hip_atomic_fetch_add(lflag + F_W, 1u, RLX_WG);
    if (!slow && !active) return;
    f32x4 acc[4], o[4];
#pragma unroll
    for (int c = 0; c < 4; ++c) { acc[c] = 0.f; o[c] = 0.f; }
    int ns = 0;
    {
        const u32x4 rsrc = make_rsrc(x - 4096, (u32)NN * 128u + 4096u);
        if (__builtin_expect(!slow, 1)) {
            ns = make_tab(smem, wv, lane);
            gather_group<false, true>(acc, rsrc, smem, wv, ns, L, make_prime(smem, wv, L), node0);
        } else {
            slow_bucket<false>(smem, acc, bl, ovf, hdr, epoch, bucket, rsrc, t, wv, L);
            if (active) own_rows(rsrc, lds_addr(ring), node0, lane);
        }
    }
    STAMP(0, 5);
    Prime P2;
    u32x2 xf[4];
    if (active) {
        u32x2 tf[4];
#pragma unroll
        for (int c = 0; c < 4; ++c) {
            tf[c][0] = pack_bf16(acc[c][0], acc[c][1]);
            tf[c][1] = pack_bf16(acc[c][2], acc[c][3]);
            acc[c] = 0.f;
        }
        store_tile(t1, node0, nrows, tf, ring + (~ns & 1) * RINGH, lane);
        lds_wait(lflag + F_W, 3u);
        dense(o, smem, 1, tf, L);
        WAIT_VM(0);
        u32 old = 0;
        if (lane == 0) old = __hip_atomic_fetch_add(lflag + F_ARR2, 1u, RLX_WG);
        const bool last = __builtin_amdgcn_readfirstlane(old) == (u32)(GPB - 1);
        if (last) bar_arrive(bar, lane);
        __builtin_amdgcn_sched_barrier(0);
        const int n = lane & 15, q = lane >> 4;
#pragma unroll
        for (int c = 0; c < 4; ++c)
            xf[c] = *(const u32x2*)(ring + (ns & 1) * RINGH + n * 128 + (((2 * c + (q >> 1)) ^ (n >> 1)) << 4) + 8 * (q & 1));
        dense(o, smem, 0, xf, L);
        P2 = make_prime(smem, wv, L);
        asm volatile("" : "+v"(P2.off[0]), "+v"(P2.off[1]), "+v"(P2.bq[0]), "+v"(P2.bq[1]), "+v"(P2.an));
        if (last) {
            bar_wait(bar, lane);
            if (lane == 0) __hip_atomic_store(lflag + F_GO2, 1u, RLX_WG);
            if (blk == NBK - 1 && lane == 0) __hip_atomic_store(bar + 288 * 64, epoch, RLX_AGENT);
        }
    }
    lds_wait(lflag + F_GO2, 1u);
    STAMP(0, 6);
    {
        const u32x4 rsrc = make_rsrc(t1 - 4096, (u32)NN * 128u + 4096u);
        if (__builtin_expect(!slow, 1)) gather_group<true, false>(acc, rsrc, smem, wv, ns, L, P2, 0);
        else slow_bucket<true>(smem, acc, bl, ovf, hdr, epoch, bucket, rsrc, t, wv, L);
    }
    if (!active) return;
    u32x2 f[4];
#pragma unroll
    for (int c = 0; c < 4; ++c) {
        f[c][0] = pack_bf16(2.f * acc[c][0] - bf_lo(xf[c][0]), 2.f * acc[c][1] - bf_hi(xf[c][0]));
        f[c][1] = pack_bf16(2.f * acc[c][2] - bf_lo(xf[c][1]), 2.f * acc[c][3] - bf_hi(xf[c][1]));
    }
    dense(o, smem, 2, f, L);
#pragma unroll
    for (int c = 0; c < 4; ++c) {
        f[c][0] = pack_bf16(o[c][0], o[c][1]);
        f[c][1] = pack_bf16(o[c][2], o[c][3]);
    }
    store_tile(out, node0, nrows, f, ring, lane);
}

extern "C" void kernel_launch(void* const* d_in, const int* in_sizes, int n_in, void* d_out, int out_size, void* d_ws,
                              size_t ws_size, hipStream_t stream) {
    k_cheb<<<NBK, 1024, 0, stream>>>((const u32*)d_in[1], (const u32*)d_in[2], (const u16*)d_in[3],
                                     (const char*)d_in[0], (const char*)d_in[4], (char*)d_ws, (char*)d_out);
}
```

```cpp
#include <hip/hip_runtime.h>
#include <hip/hip_bf16.h>
#include <stdint.h>

typedef unsigned int u32;
typedef unsigned short u16;
typedef unsigned char u8;
typedef u32 u32x4 __attribute__((ext_vector_type(4)));
typedef u32 u32x2 __attribute__((ext_vector_type(2)));
typedef short s16x2 __attribute__((ext_vector_type(2)));
typedef short s16x4 __attribute__((ext_vector_type(4)));
typedef __bf16 bf16x8 __attribute__((ext_vector_type(8)));
typedef __bf16 bf16x2 __attribute__((ext_vector_type(2)));
typedef float f32x4 __attribute__((ext_vector_type(4)));
#define LAS __attribute__((address_space(3)))

constexpr int NN = 50000;
constexpr int NBK = 256;
constexpr int NB1 = 250, CH = 3200;
constexpr int GPB = 13;
constexpr int SLOTS = 24;
constexpr int CAP2 = 192;

#define DELAY 50
constexpr int NSH = 4;
constexpr int NREP = 64;

constexpr int L_LO = 0;
constexpr int L_NUL = L_LO + GPB * 2 * CAP2 * 4;
constexpr int L_DUMP = L_NUL + 32;
constexpr int L_OH = L_DUMP + 32;
constexpr int L_CNT = L_OH + L_OH / 2;
constexpr int L_TAB = L_CNT + 256;
constexpr int L_W = (L_TAB + GPB * 128 + 255) / 256 * 256;
constexpr int L_RING = L_W + 24576;
constexpr int RINGH = GPB * 4096;
constexpr int L_K2 = L_RING + 2 * RINGH;
constexpr int L_STAGE = L_RING;
constexpr int L_CNTA = L_STAGE + 32768;
static_assert(L_K2 <= 163840 && 32768 + 1040 <= RINGH, "lds");
static_assert(L_OH % 32 == 0 && L_CNT % 16 == 0 && L_TAB % 16 == 0, "align");
enum { F_BC = 56, F_DONE = 59, F_ARR2 = 60, F_W = 61, F_GO2 = 62 };

constexpr size_t W_LINE = 0;
constexpr size_t W_OVF = W_LINE + (size_t)NBK * NB1 * 128;
constexpr size_t W_HDR = W_OVF + (size_t)NB1 * CH * 16;
constexpr size_t W_BAR = W_HDR + 2048;
constexpr size_t W_T1 = W_BAR + 512 * 256;
static_assert(W_OVF % 128 == 0 && W_BAR % 256 == 0 && W_T1 % 128 == 0 && NREP * NSH == 256, "align");
constexpr u32 HKEY = 0x9e3779b9u;
constexpr unsigned long long TMO = 20000000ull;

__device__ __forceinline__ u32 lds_addr(const void* p) { return (u32)(uintptr_t)(const LAS char*)p; }
__device__ __forceinline__ u32 bstart(u32 b) { return (b * 3125u) >> 4; }
__device__ __forceinline__ u32 bucket_of(u32 d) { return __umulhi(d * 16u + 15u, 1374390u); }

__device__ __forceinline__ u32x4 make_rsrc(const void* base, u32 bytes) {
    const uint64_t a = (uint64_t)base;
    u32x4 r;
    r[0] = (u32)a;
    r[1] = (u32)(a >> 32) & 0xffffu;
    r[2] = bytes;
    r[3] = 0x00020000u;
    return r;
}

template <bool SC1>
__device__ __forceinline__ void dma4(u32x4 rsrc, u32 lds, u32 o0, u32 o1, u32 o2, u32 o3) {
    u32 keep;
    if (SC1)
        asm volatile(
            "s_waitcnt lgkmcnt(0)\n\t"
            "s_mov_b32 %0, m0\n\t"
            "s_mov_b32 m0, %1\n\t"
            "s_nop 0\n\t"
            "buffer_load_dwordx4 %2, %6, 0 offen sc1 lds\n\t"
            "buffer_load_dwordx4 %3, %6, 0 offen offset:1024 sc1 lds\n\t"
            "buffer_load_dwordx4 %4, %6, 0 offen offset:2048 sc1 lds\n\t"
            "buffer_load_dwordx4 %5, %6, 0 offen offset:3072 sc1 lds\n\t"
            "s_mov_b32 m0, %0"
            : "=&s"(keep)
            : "s"(lds), "v"(o0), "v"(o1), "v"(o2), "v"(o3), "s"(rsrc)
            : "memory");
    else
        asm volatile(
            "s_waitcnt lgkmcnt(0)\n\t"
            "s_mov_b32 %0, m0\n\t"
            "s_mov_b32 m0, %1\n\t"
            "s_nop 0\n\t"
            "buffer_load_dwordx4 %2, %6, 0 offen lds\n\t"
            "buffer_load_dwordx4 %3, %6, 0 offen offset:1024 lds\n\t"
            "buffer_load_dwordx4 %4, %6, 0 offen offset:2048 lds\n\t"
            "buffer_load_dwordx4 %5, %6, 0 offen offset:3072 lds\n\t"
            "s_mov_b32 m0, %0"
            : "=&s"(keep)
            : "s"(lds), "v"(o0), "v"(o1), "v"(o2), "v"(o3), "s"(rsrc)
            : "memory");
}
__device__ __forceinline__ void dma2(u32x4 rsrc, u32 lds, u32 o0, u32 o1) {
    u32 keep;
    asm volatile(
        "s_waitcnt lgkmcnt(0)\n\t"
        "s_mov_b32 %0, m0\n\t"
        "s_mov_b32 m0, %1\n\t"
        "s_nop 0\n\t"
        "buffer_load_dwordx4 %2, %4, 0 offen lds\n\t"
        "buffer_load_dwordx4 %3, %4, 0 offen offset:1024 lds\n\t"
        "s_mov_b32 m0, %0"
        : "=&s"(keep)
        : "s"(lds), "v"(o0), "v"(o1), "s"(rsrc)
        : "memory");
}
#define WAIT_VM(n) asm volatile("s_waitcnt vmcnt(" #n ")" ::: "memory")
#define RLX_AGENT __ATOMIC_RELAXED, __HIP_MEMORY_SCOPE_AGENT
#define RLX_WG __ATOMIC_RELAXED, __HIP_MEMORY_SCOPE_WORKGROUP
__device__ __forceinline__ u32x4 ld16(const void* base, u32 bytes, u32 off) {
    return __builtin_amdgcn_raw_buffer_load_b128(__builtin_amdgcn_make_buffer_rsrc((void*)base, 0, bytes, 0x00020000), off, 0, 16);
}
__device__ __forceinline__ void st16(void* p, u32x4 v) {
    asm volatile("global_store_dwordx4 %0, %1, off sc1\n\ts_nop 1" ::"v"(p), "v"(v) : "memory");
}

#define STAMP_DECL do {} while (0)
#define STAMP(k, i) do {} while (0)
#define STAMP_FLUSH(k) do {} while (0)

__device__ __forceinline__ u32 pack_bf16(float a, float b) {
    bf16x2 v;
    v[0] = (__bf16)a;
    v[1] = (__bf16)b;
    return __builtin_bit_cast(u32, v);
}
__device__ __forceinline__ float bf_lo(u32 v) { return __uint_as_float(v << 16); }
__device__ __forceinline__ float bf_hi(u32 v) { return __uint_as_float(v & 0xffff0000u); }

struct Lane {
    u32 coff[4];
    u32 tra[4];
    u32 sh;
    int lane;
};
__device__ __forceinline__ Lane make_lane(int lane) {
    Lane L;
    L.lane = lane;
    const int a = lane >> 3, p = lane & 7;
#pragma unroll
    for (int i = 0; i < 4; ++i) L.coff[i] = (u32)(((p ^ ((((a >> 1) & 1) << 1) | ((i & 1) << 2))) << 4) + 4096 - 1024 * i);
    const int g = lane >> 4, q = (lane & 15) >> 2, pp = lane & 3;
    const int sw = ((g & 1) << 1) | ((q & 1) << 2);
#pragma unroll
    for (int c = 0; c < 4; ++c)
        L.tra[c] = (u32)(128 * (8 * (q & 1) + 2 * g + (q >> 1)) + 16 * ((2 * c + (pp >> 1)) ^ sw) + 8 * (pp & 1));
    L.sh = (u32)(15 - (lane & 15)) * 0x10001u;
    return L;
}
__device__ __forceinline__ bf16x8 afrag(const char* buf, const Lane& L, int c) {
    const s16x4 a0 = __builtin_amdgcn_ds_read_tr16_b64_v4i16((LAS s16x4*)(buf + L.tra[c]));
    const s16x4 a1 = __builtin_amdgcn_ds_read_tr16_b64_v4i16((LAS s16x4*)(buf + L.tra[c] + 2048));
    return __builtin_bit_cast(bf16x8, __builtin_shufflevector(a0, a1, 0, 1, 2, 3, 4, 5, 6, 7));
}
__device__ __forceinline__ u32 row_off(u32 lo, u32 add) {
    u32 r;
    asm("v_mad_u32_u16 %0, %1, %2, %3" : "=v"(r) : "v"(lo), "s"(128u), "v"(add));
    return r;
}
__device__ __forceinline__ u32 sel2(u32 oh, u32 lo0, u32 lo1, u32 sh) {
    const s16x2 m = (__builtin_bit_cast(s16x2, oh) << __builtin_bit_cast(s16x2, sh)) >> 15;
    return __builtin_amdgcn_perm(lo1, lo0, 0x07060302u) & __builtin_bit_cast(u32, m);
}
__device__ __forceinline__ int make_tab(char* smem, int wv, int lane) {
    const u32x4 cv = *(const u32x4*)(smem + L_CNT + 16 + wv * 16);
    const u32 c0 = __builtin_amdgcn_readfirstlane(cv[0]), c1 = __builtin_amdgcn_readfirstlane(cv[1]);
    const u32 c2 = __builtin_amdgcn_readfirstlane(cv[2]), c3 = __builtin_amdgcn_readfirstlane(cv[3]);
    const u32 b0 = (u32)wv * 48u;
    const u32 n0 = (c0 + 31u) >> 5, s1 = max((c1 + 4u) >> 5, n0);
    const u32 e2 = (c2 + 31u) >> 5, s3 = max((c3 + 4u) >> 5, e2);
    const u32 ea = n0 - b0, eb = ea + (e2 - s1), nb = eb + (b0 + 48u - s3), v = (u32)lane;
    u32 pb = v < eb ? s1 - ea : s3 - eb;
    pb = v < ea ? b0 : pb;
    const u32 a = v < nb ? (pb + v) * 32u : (u32)L_NUL;
    *(u16*)(smem + L_TAB + wv * 128 + lane * 2) = (u16)a;
    __builtin_amdgcn_wave_barrier();
    return (int)((nb + 3u) >> 2);
}
struct Blk { u32x4 ix, l0, l1, oh; };
__device__ __forceinline__ Blk read_blk(const char* smem, u32 a, const Lane& L) {
    Blk B;
    B.ix = *(const u32x4*)(smem + a + (L.lane & 8) * 2);
    B.l0 = *(const u32x4*)(smem + a);
    B.l1 = *(const u32x4*)(smem + a + 16);
    B.oh = *(const u32x4*)(smem + L_OH + (a >> 1));
    return B;
}
template <bool SC1>
__device__ __forceinline__ void issue_blk(u32x4 rsrc, const Blk& B, u32 buf, const Lane& L) {
    dma4<SC1>(rsrc, buf, row_off(B.ix[0], L.coff[0]), row_off(B.ix[1], L.coff[1]), row_off(B.ix[2], L.coff[2]), row_off(B.ix[3], L.coff[3]));
}
__device__ __forceinline__ u32x4 sel_blk(const Blk& B, const Lane& L) {
    u32x4 b;
    b[0] = sel2(B.oh[0], B.l0[0], B.l0[1], L.sh);
    b[1] = sel2(B.oh[2], B.l1[0], B.l1[1], L.sh);
    b[2] = sel2(B.oh[1], B.l0[2], B.l0[3], L.sh);
    b[3] = sel2(B.oh[3], B.l1[2], B.l1[3], L.sh);
    return b;
}
__device__ __forceinline__ void own_rows(u32x4 rsrc, u32 buf, int node0, int lane) {
    const u32 o = (u32)(node0 + (lane >> 3)) * 128u + (u32)(((lane & 7) ^ (lane >> 4)) << 4) + 4096u;
    dma2(rsrc, buf, o, o ^ 64u);
}
struct Prime { u32x4 off[2], bq[2]; u32 an; };
__device__ __forceinline__ Prime make_prime(const char* smem, int wv, const Lane& L) {
    const u16* tp = (const u16*)(smem + L_TAB + wv * 128) + (L.lane >> 4);
    Prime P;
#pragma unroll
    for (int h = 0; h < 2; ++h) {
        const Blk B = read_blk(smem, tp[4 * h], L);
#pragma unroll
        for (int i = 0; i < 4; ++i) P.off[h][i] = row_off(B.ix[i], L.coff[i]);
        P.bq[h] = sel_blk(B, L);
    }
    P.an = tp[8];
    return P;
}
template <bool SC1, bool OWN>
__device__ __forceinline__ void gather_group(f32x4 (&acc)[4], u32x4 rsrc, char* smem, int wv, int ns, const Lane& L, const Prime& P,
                                             int node0) {
    char* ring = smem + L_RING + wv * 4096;
    const u32 rb = lds_addr(ring);
    const u16* tp = (const u16*)(smem + L_TAB + wv * 128) + (L.lane >> 4);
    u32x4 bq[2] = {P.bq[0], P.bq[1]};
    __builtin_amdgcn_s_setprio(3);
#pragma unroll
    for (int h = 0; h < 2; ++h)
        if (h < ns) dma4<SC1>(rsrc, rb + h * RINGH, P.off[h][0], P.off[h][1], P.off[h][2], P.off[h][3]);
        else if (OWN && h == ns) own_rows(rsrc, rb + h * RINGH, node0, L.lane);
    u32 an = P.an;
    int rem = ns;
    while (rem > 0) {
#pragma unroll
        for (int h = 0; h < 2; ++h) {
            if (h == 1 && rem == 0) break;
            if (rem == 7) __builtin_amdgcn_s_setprio(2);
            else if (rem == 5) __builtin_amdgcn_s_setprio(1);
            else if (rem == 3) __builtin_amdgcn_s_setprio(0);
            const Blk B = read_blk(smem, an, L);
            an = tp[4 * (h + 3)];
            if (rem > 1) WAIT_VM(4); else if (OWN) WAIT_VM(2); else WAIT_VM(0);
            bf16x8 a[4];
#pragma unroll
            for (int c = 0; c < 4; ++c) a[c] = afrag(ring + h * RINGH, L, c);
            if (rem > 2) issue_blk<SC1>(rsrc, B, rb + h * RINGH, L);
            else if (OWN && rem == 2) own_rows(rsrc, rb + h * RINGH, node0, L.lane);
#pragma unroll
            for (int c = 0; c < 4; ++c)
                acc[c] = __builtin_amdgcn_mfma_f32_16x16x32_bf16(a[c], __builtin_bit_cast(bf16x8, bq[h]), acc[c], 0, 0, 0);
            bq[h] = sel_blk(B, L);
            --rem;
        }
        tp += 8;
    }
    __builtin_amdgcn_s_setprio(0);
}

__device__ __forceinline__ void put_record(char* smem, u32 lo, u32 meta) {
    const u32 q = ((lo & 0xffffu) * 1342u) >> 24;
    u32 a = atomicAdd((u32*)(smem + L_CNT + (meta & 0xf0u) + q * 4u), (q & 1u) ? (u32)-4 : 4u);
    a = min(a, (u32)L_DUMP);
    *(u32*)(smem + a) = lo;
    *(u16*)(smem + L_OH + (a >> 1)) = (u16)(1u << (meta & 15u));
}
struct Pos { u32 a[3], oh[3]; bool ok[3]; };
__device__ __forceinline__ Pos claim_chunk(char* smem, u32x4 v) {
    Pos P;
#pragma unroll
    for (int k = 0; k < 3; ++k) {
        const u32 g = (v[3] >> (8 * k)) & 0xf0u;
        const u32 m = (v[k] & 0xffffu) * 1342u;
        u32 dir;
        asm("v_bfe_i32 %0, %1, 24, 1\n\tv_lshl_or_b32 %0, %0, 3, 4" : "=v"(dir) : "v"(m));
        if (k == 0) asm("v_lshlrev_b16_sdwa %0, %1, %2 dst_sel:DWORD dst_unused:UNUSED_PAD src0_sel:BYTE_0 src1_sel:DWORD" : "=v"(P.oh[k]) : "v"(v[3]), "v"(1u));
        if (k == 1) asm("v_lshlrev_b16_sdwa %0, %1, %2 dst_sel:DWORD dst_unused:UNUSED_PAD src0_sel:BYTE_1 src1_sel:DWORD" : "=v"(P.oh[k]) : "v"(v[3]), "v"(1u));
        if (k == 2) asm("v_lshlrev_b16_sdwa %0, %1, %2 dst_sel:DWORD dst_unused:UNUSED_PAD src0_sel:BYTE_2 src1_sel:DWORD" : "=v"(P.oh[k]) : "v"(v[3]), "v"(1u));
        P.ok[k] = g != 0u;
        P.a[k] = dir;
        if (P.ok[k]) P.a[k] = atomicAdd((u32*)(smem + L_CNT + (g | ((m >> 22) & 0xcu))), dir);
    }
    return P;
}
__device__ __forceinline__ void write_chunk(char* smem, Pos& P, u32x4 v) {
    asm volatile("" : "+v"(P.a[0]), "+v"(P.a[1]), "+v"(P.a[2]));
#pragma unroll
    for (int k = 0; k < 3; ++k) {
        const u32 p = min(P.a[k], (u32)L_DUMP);
        if (P.ok[k]) {
            *(u32*)(smem + p) = v[k];
            *(u16*)(smem + L_OH + (p >> 1)) = (u16)P.oh[k];
        }
    }
}
__device__ __forceinline__ void put_surplus(char* smem, u32x4 r, u32 bucket) {
    if (bucket_of(r[1]) == bucket) put_record(smem, r[0], r[1] - bstart(bucket) + 16u);
}
__device__ __forceinline__ u32 wait_hdr(const unsigned long long* hdr, int b, u32 epoch) {
    const unsigned long long t0 = __builtin_amdgcn_s_memrealtime();
    for (;;) {
        const unsigned long long h = __hip_atomic_load(hdr + b, RLX_AGENT);
        if (__builtin_amdgcn_readfirstlane((u32)(h >> 32)) == (epoch ^ HKEY)) return min((u32)__builtin_amdgcn_readfirstlane((u32)h), (u32)CH);
        if (__builtin_amdgcn_s_memrealtime() - t0 > TMO) return 0u;
        __builtin_amdgcn_s_sleep(2);
    }
}
template <bool FIRST>
__device__ __forceinline__ void init_lists(char* smem, int t) {
#pragma unroll
    for (int h = 0; h < 2; ++h) {
        const int i = t + h * 1024;
        if (i < L_CNT / 16) *(u32x4*)(smem + i * 16) = (u32x4)(i < L_OH / 16 ? 0xffffffffu : 0u);
    }
    if (t >= 960) {
        const u32 b = (u32)t - 960u - 4u;
        if (b < 4u * GPB) *(u32*)(smem + L_CNT + 16 + b * 4) = (b >> 1) * (CAP2 * 4u) + ((b & 1u) ? (CAP2 - 1) * 4u : 0u);
        else if (FIRST) *(u32*)(smem + L_CNT + 16 + (int)b * 4) = 0u;
    }
}

template <bool SC1>
__device__ __forceinline__ void slow_bucket(char* smem, f32x4 (&acc)[4], const char* bl, const char* ovf,
                                            const unsigned long long* hdr, u32 epoch, u32 bucket,
                                            u32x4 rsrc, int t, int wv, const Lane& L) {
    int blk = -((NB1 + 7) / 8);
    u32 o0 = 0, n = 0;
    for (;;) {
        if (blk >= 0 && o0 >= n) {
            u32* bc = (u32*)(smem + L_CNT) + F_BC;
            __syncthreads();
            if (wv == 0) {
                for (; blk < NB1; ++blk)
                    if (__any(__hip_atomic_load((const u32*)(bl + blk * 128 + (t & 7) * 16 + 12), RLX_AGENT) >> 31)) break;
                n = blk < NB1 ? wait_hdr(hdr, blk, epoch) : 0u;
                if (t == 0) { bc[0] = (u32)blk; bc[1] = n; }
            }
            __syncthreads();
            blk = __builtin_amdgcn_readfirstlane((int)bc[0]);
            n = __builtin_amdgcn_readfirstlane(bc[1]);
            if (blk >= NB1) break;
            o0 = 0;
        }
        __syncthreads();
        init_lists<false>(smem, t);
        __syncthreads();
        if (blk < 0) {
            const int c = (blk + (NB1 + 7) / 8) * 64 + t;
            if (t < 64 && c < NB1 * 8) { const u32x4 v = ld16(bl, NB1 * 128, c * 16); Pos P = claim_chunk(smem, v); write_chunk(smem, P, v); }
            ++blk;
        } else {
            if (t < CAP2 && o0 + t < n) put_surplus(smem, ld16(ovf, NB1 * CH * 16, (blk * CH + o0 + t) * 16), bucket);
            o0 += CAP2;
            if (o0 >= n) ++blk;
        }
        __syncthreads();
        if (wv < GPB) {
            const int ns = make_tab(smem, wv, L.lane);
            gather_group<SC1, false>(acc, rsrc, smem, wv, ns, L, make_prime(smem, wv, L), 0);
        }
    }
}

__device__ __forceinline__ void store_tile(char* __restrict__ outp, int node0, int nrows, const u32x2 (&v)[4], char* buf, int lane) {
    const int n = lane & 15, q = lane >> 4;
#pragma unroll
    for (int c = 0; c < 4; ++c) *(u32x2*)(buf + n * 128 + (((2 * c + (q >> 1)) ^ (n & 7)) << 4) + 8 * (q & 1)) = v[c];
    __builtin_amdgcn_wave_barrier();
    char* p = outp + (size_t)node0 * 128 + lane * 16;
    const int r = lane >> 3;
#pragma unroll
    for (int h = 0; h < 2; ++h)
        if (r + 8 * h < nrows) st16(p + h * 1024, *(const u32x4*)(buf + h * 1024 + r * 128 + (((lane & 7) ^ r) << 4)));
}
__device__ __forceinline__ void dense(f32x4 (&o)[4], const char* smem, int k, const u32x2 (&f)[4], const Lane& L) {
#pragma unroll
    for (int kk = 0; kk < 2; ++kk) {
        u32x4 b;
        b[0] = f[2 * kk][0]; b[1] = f[2 * kk][1]; b[2] = f[2 * kk + 1][0]; b[3] = f[2 * kk + 1][1];
#pragma unroll
        for (int c = 0; c < 4; ++c)
            o[c] = __builtin_amdgcn_mfma_f32_16x16x32_bf16(afrag(smem + L_W + (2 * k + kk) * 4096, L, c), __builtin_bit_cast(bf16x8, b), o[c], 0, 0, 0);
    }
}

__device__ __forceinline__ void bar_arrive(u32* bar, int lane) {
    if (lane < NREP) __hip_atomic_fetch_add(bar + (lane * NSH + blockIdx.x % NSH) * 64, 1u, RLX_AGENT);
}
__device__ __forceinline__ void bar_wait(const u32* bar, int lane) {
    const u32* pb = bar + ((blockIdx.x / NSH) % NREP * NSH + (lane % NSH)) * 64;
    const unsigned long long t0 = __builtin_amdgcn_s_memrealtime();
    u32 c = lane < NSH ? 0u : (u32)(NBK / NSH);
    while (__builtin_amdgcn_s_memrealtime() - t0 < TMO) {
        if (c != (u32)(NBK / NSH)) c = __hip_atomic_load(pb, RLX_AGENT);
        if (__all(c == (u32)(NBK / NSH))) break;
        __builtin_amdgcn_s_sleep(2);
    }
}
__device__ __forceinline__ void lds_wait(const u32* f, u32 n) {
    while (__hip_atomic_load(f, RLX_WG) < n) __builtin_amdgcn_s_sleep(1);
    asm volatile("" ::: "memory");
}

__device__ __forceinline__ void wait_lines(u32x4& v, const char* bl, u32 off, u32 tag, int lane) {
    const bool in = off < (u32)(NB1 * 128);
    unsigned long long bad = __ballot(in && ((v[3] >> 24) & 0x7fu) != tag);
    if (bad != 0ull) {
        const unsigned long long t0 = __builtin_amdgcn_s_memrealtime();
        do {
            __builtin_amdgcn_s_sleep(2);
            asm volatile("" ::: "memory");
            if ((bad >> (lane & 56)) & 0xffull) v = ld16(bl, NB1 * 128, off);
            asm volatile("" : "+v"(v));
            bad = __ballot(in && ((v[3] >> 24) & 0x7fu) != tag);
        } while (bad != 0ull && __builtin_amdgcn_s_memrealtime() - t0 < TMO);
        if (bad != 0ull) v[3] = 0u;
    }
}
__device__ __forceinline__ u32x4 first_surplus(unsigned long long f, int b0, const char* ovf, int lane) {
    return ld16(ovf, NB1 * CH * 16, (u32)((b0 + (__builtin_ctzll(f) >> 3)) * CH + lane) * 16u);
}
__device__ __forceinline__ void take_surplus(char* smem, unsigned long long f, int b0, u32x4 r, const char* ovf,
                                             const unsigned long long* hdr, u32 epoch, u32 bucket, int lane) {
    for (;;) {
        const int j = __builtin_ctzll(f) >> 3, b = b0 + j;
        const u32 m = (u32)(f >> (8 * j)) & 0xffu;
        bool done = false;
        for (int it = 0; m < 64u; ++it) {
            const bool mine = r[2] == epoch && r[3] == ~epoch && bucket_of(r[1] & 0xffffu) == bucket;
            if ((u32)__builtin_popcountll(__ballot(mine)) == m) {
                if (mine) put_surplus(smem, r, bucket);
                done = true;
                break;
            }
            if (it == 3) break;
            asm volatile("" ::: "memory");
            r = first_surplus(f, b0, ovf, lane);
        }
        if (!done) {
            const u32 n = wait_hdr(hdr, b, epoch);
            for (u32 o = lane; o < n; o += 64) put_surplus(smem, ld16(ovf, NB1 * CH * 16, (b * CH + o) * 16u), bucket);
        }
        f &= ~(0xffull << (8 * j));
        if (f == 0ull) break;
        r = first_surplus(f, b0, ovf, lane);
    }
}

__global__ __launch_bounds__(1024, 4) void k_cheb(const u32* __restrict__ src, const u32* __restrict__ dst,
                                               const u16* __restrict__ ewp, const char* __restrict__ x,
                                               const char* __restrict__ w, char* ws, char* __restrict__ out) {
    __shared__ __attribute__((aligned(256))) char smem[L_K2];
    char* lines = ws + W_LINE;
    char* ovf = ws + W_OVF;
    unsigned long long* hdr = (unsigned long long*)(ws + W_HDR);
    u32* bar = (u32*)(ws + W_BAR);
    char* t1 = ws + W_T1;
    const int t = threadIdx.x, lane = t & 63, wv = __builtin_amdgcn_readfirstlane(t >> 6);
    const int blk = blockIdx.x;
    const int xr = blk >> 3;
    const int bucket = (blk & 7) * 32 + xr;
    const bool active = wv < GPB;
    const int node0 = (int)bstart(bucket) + wv * 16;
    const int nrows = min((int)bstart(bucket + 1) - node0, 16);
    char* ring = smem + L_RING + wv * 4096;
    u32* lflag = (u32*)(smem + L_CNT);
    Lane L = make_lane(lane);
    STAMP_DECL;
    STAMP(0, 0);
    u32 epoch;
    {
        u32* stage = (u32*)(smem + L_STAGE);
        u32* cnt = (u32*)(smem + L_CNTA);
        const u32 nb = blk < NB1 ? (u32)CH : 0u;
        const size_t e0i = (size_t)min(blk, NB1 - 1) * CH;
        const u32x4 d = __builtin_amdgcn_raw_buffer_load_b128(__builtin_amdgcn_make_buffer_rsrc((void*)(dst + e0i), 0, nb * 4u, 0x00020000), t * 16, 0, 2);
        const u32x4 s = __builtin_amdgcn_raw_buffer_load_b128(__builtin_amdgcn_make_buffer_rsrc((void*)(src + e0i), 0, nb * 4u, 0x00020000), t * 16, 0, 2);
        const u32x2 ew = __builtin_amdgcn_raw_buffer_load_b64(__builtin_amdgcn_make_buffer_rsrc((void*)(ewp + e0i), 0, nb * 2u, 0x00020000), t * 8, 0, 2);
        const u32 e1 = __hip_atomic_load(bar + 288 * 64, RLX_AGENT) + 1u;
        if (blk == NBK - 1 && t < 256) __hip_atomic_store(bar + t * 64, 0u, RLX_AGENT);
        if (t < 512) *(u32x4*)&stage[3 * 2048 + t * 4] = (u32x4)0u;
        else if (t < 512 + 65) *(u32x4*)&cnt[(t - 512) * 4] = (u32x4)0u;
        init_lists<true>(smem, t);
        asm volatile("" : "+v"(L.coff[0]), "+v"(L.coff[1]), "+v"(L.coff[2]), "+v"(L.coff[3]), "+v"(L.tra[0]), "+v"(L.tra[1]),
                     "+v"(L.tra[2]), "+v"(L.tra[3]), "+v"(L.sh));
        __syncthreads();
        __builtin_amdgcn_sched_barrier(0);
        const bool bin = t < CH / 4 && blk < NB1;
        u32 rk[4], bk[4];
#pragma unroll
        for (int i = 0; i < 4; ++i) {
            bk[i] = bucket_of((u32)d[i]);
            rk[i] = 0u;
            if (bin) rk[i] = atomicAdd(&cnt[bk[i]], 1u);
        }
        asm volatile("" :: "v"(s), "v"(ew));
        if (bin) {
#pragma unroll
            for (int i = 0; i < 4; ++i) {
                const u32 lo = (u32)s[i] | ((i & 1) ? (ew[i >> 1] & 0xffff0000u) : (ew[i >> 1] << 16));
                const u32 c = bk[i] * 8u + (rk[i] & 7u), sub = rk[i] >> 3;
                if (__builtin_expect(rk[i] < (u32)SLOTS, 1)) {
                    stage[sub * 2048u + c] = lo;
                    ((u8*)&stage[3 * 2048 + c])[sub] = (u8)((u32)d[i] - bstart(bk[i]) + 16u);
                } else {
                    const u32 o = atomicAdd(&cnt[256], 1u);
                    u32x4 r;
                    r[0] = lo; r[1] = (u32)d[i]; r[2] = e1; r[3] = ~e1;
                    st16(ovf + (size_t)(blk * CH + o) * 16, r);
                }
            }
        }
        epoch = __builtin_amdgcn_readfirstlane(e1);
        if (blk == NBK - 1) WAIT_VM(0);
        STAMP(0, 1);
        __syncthreads();
        if (blk < NB1) {
#pragma unroll
            for (int h = 0; h < 2; ++h) {
                const int c = t + h * 1024;
                const u32 m = min(max(cnt[c >> 3], (u32)SLOTS) - (u32)SLOTS, 255u);
                u32x4 v;
                v[0] = stage[c]; v[1] = stage[2048 + c]; v[2] = stage[4096 + c];
                v[3] = stage[6144 + c] | ((epoch & 0x7fu) << 24) | ((m >> (c & 7)) << 31);
                st16(lines + ((size_t)(c >> 3) * NB1 + blk) * 128 + (c & 7) * 16, v);
            }
        } else if (blk == NBK - 1 && t < 32) {
            __hip_atomic_store((unsigned long long*)(bar + (256 + t) * 64), (unsigned long long)epoch | ((unsigned long long)(epoch ^ HKEY) << 32), RLX_AGENT);
        }
    }
    STAMP(0, 2);
    const char* bl = lines + (size_t)bucket * (NB1 * 128);
    {
        __builtin_amdgcn_s_sleep(DELAY);
        if (blk >= NB1) __builtin_amdgcn_s_sleep(30);
        u32x4 v0 = ld16(bl, NB1 * 128, t * 16);
        u32x4 v1 = ld16(bl, NB1 * 128, (t + 1024) * 16);
        u32x4 r0 = 0u, r1 = 0u;
        wait_lines(v0, bl, t * 16, epoch & 0x7fu, lane);
        const unsigned long long f0 = __ballot(v0[3] >> 31);
        if (__builtin_expect(f0 != 0ull, 0)) r0 = first_surplus(f0, wv * 8, ovf, lane);
        {
            Pos P0 = claim_chunk(smem, v0);
            write_chunk(smem, P0, v0);
        }
        __builtin_amdgcn_sched_barrier(0);
        wait_lines(v1, bl, (t + 1024) * 16, epoch & 0x7fu, lane);
        STAMP(0, 3);
        const unsigned long long f1 = __ballot(v1[3] >> 31);
        if (__builtin_expect(f1 != 0ull, 0)) r1 = first_surplus(f1, wv * 8 + 128, ovf, lane);
        {
            Pos P1 = claim_chunk(smem, v1);
            write_chunk(smem, P1, v1);
        }
        {
            u32* cnt = (u32*)(smem + L_CNTA);
            const u32 novf = cnt[256];
            if (__builtin_expect(novf != 0u, 0)) {
                WAIT_VM(0);
                u32 old = 0;
                if (lane == 0) old = atomicAdd(&cnt[257], 1u);
                if (old == 15u) __hip_atomic_store(hdr + blk, (unsigned long long)novf | ((unsigned long long)(epoch ^ HKEY) << 32), RLX_AGENT);
            }
        }
        if (__builtin_expect(f0 != 0ull, 0)) take_surplus(smem, f0, wv * 8, r0, ovf, hdr, epoch, bucket, lane);
        if (__builtin_expect(f1 != 0ull, 0)) take_surplus(smem, f1, wv * 8 + 128, r1, ovf, hdr, epoch, bucket, lane);
    }
    __syncthreads();
    STAMP(0, 4);
    if (!active) {
        __builtin_amdgcn_s_setprio(3);
        const u32x4 wrs = make_rsrc(w - 4096, 3u * 64u * 128u + 4096u);
        const int a = lane >> 3;
#pragma unroll
        for (int h = 0; h < 2; ++h) {
            const int ks = (wv - GPB) * 2 + h;
            const u32 rb = (u32)(32 * ks + 4 * (a >> 1) + 2 * (a & 1)) * 128u;
            dma4<false>(wrs, lds_addr(smem + L_W + ks * 4096), rb + L.coff[0], rb + 128u + L.coff[1], rb + 2048u + L.coff[2], rb + 2176u + L.coff[3]);
        }
    }
    bool slow;
    {
        const u32x2 c = *(const u32x2*)(smem + L_CNT + 16 + min(lane, 2 * GPB - 1) * 8);
        slow = __builtin_expect(__any((int)c[0] > (int)c[1] + 4), 0);
    }
    if (!active) WAIT_VM(0);
    if (wv == 15) {
        const unsigned long long* rp = (const unsigned long long*)(bar + (256 + xr) * 64);
        const unsigned long long t0 = __builtin_amdgcn_s_memrealtime();
        while (__hip_atomic_load(rp, RLX_AGENT) != ((unsigned long long)epoch | ((unsigned long long)(epoch ^ HKEY) << 32)) &&
               __builtin_amdgcn_s_memrealtime() - t0 < TMO) __builtin_amdgcn_s_sleep(4);
    }
    if (!active && lane == 0) __hip_atomic_fetch_add(lflag + F_W, 1u, RLX_WG);
    if (!slow && !active) return;
    f32x4 acc[4], o[4];
#pragma unroll
    for (int c = 0; c < 4; ++c) { acc[c] = 0.f; o[c] = 0.f; }
    int ns = 0;
    {
        const u32x4 rsrc = make_rsrc(x - 4096, (u32)NN * 128u + 4096u);
        if (__builtin_expect(!slow, 1)) {
            ns = make_tab(smem, wv, lane);
            gather_group<false, true>(acc, rsrc, smem, wv, ns, L, make_prime(smem, wv, L), node0);
        } else {
            slow_bucket<false>(smem, acc, bl, ovf, hdr, epoch, bucket, rsrc, t, wv, L);
            if (active) own_rows(rsrc, lds_addr(ring), node0, lane);
        }
    }
    STAMP(0, 5);
    Prime P2;
    u32x2 xf[4];
    if (active) {
        u32x2 tf[4];
#pragma unroll
        for (int c = 0; c < 4; ++c) {
            tf[c][0] = pack_bf16(acc[c][0], acc[c][1]);
            tf[c][1] = pack_bf16(acc[c][2], acc[c][3]);
            acc[c] = 0.f;
        }
        store_tile(t1, node0, nrows, tf, ring + (~ns & 1) * RINGH, lane);
        lds_wait(lflag + F_W, 3u);
        dense(o, smem, 1, tf, L);
        WAIT_VM(0);
        u32 old = 0;
        if (lane == 0) old = __hip_atomic_fetch_add(lflag + F_ARR2, 1u, RLX_WG);
        const bool last = __builtin_amdgcn_readfirstlane(old) == (u32)(GPB - 1);
        if (last) bar_arrive(bar, lane);
        __builtin_amdgcn_sched_barrier(0);
        const int n = lane & 15, q = lane >> 4;
#pragma unroll
        for (int c = 0; c < 4; ++c)
            xf[c] = *(const u32x2*)(ring + (ns & 1) * RINGH + n * 128 + (((2 * c + (q >> 1)) ^ (n >> 1)) << 4) + 8 * (q & 1));
        dense(o, smem, 0, xf, L);
        P2 = make_prime(smem, wv, L);
        asm volatile("" : "+v"(P2.off[0]), "+v"(P2.off[1]), "+v"(P2.bq[0]), "+v"(P2.bq[1]), "+v"(P2.an));
        if (last) {
            bar_wait(bar, lane);
            if (lane == 0) __hip_atomic_store(lflag + F_GO2, 1u, RLX_WG);
            if (blk == NBK - 1 && lane == 0) __hip_atomic_store(bar + 288 * 64, epoch, RLX_AGENT);
        }
    }
    lds_wait(lflag + F_GO2, 1u);
    STAMP(0, 6);
    {
        const u32x4 rsrc = make_rsrc(t1 - 4096, (u32)NN * 128u + 4096u);
        if (__builtin_expect(!slow, 1)) gather_group<true, false>(acc, rsrc, smem, wv, ns, L, P2, 0);
        else slow_bucket<true>(smem, acc, bl, ovf, hdr, epoch, bucket, rsrc, t, wv, L);
    }
    if (!active) return;
    if (lane == 0) __hip_atomic_fetch_add(lflag + F_DONE, 1u, RLX_WG);
    u32x2 f[4];
#pragma unroll
    for (int c = 0; c < 4; ++c) {
        f[c][0] = pack_bf16(2.f * acc[c][0] - bf_lo(xf[c][0]), 2.f * acc[c][1] - bf_hi(xf[c][0]));
        f[c][1] = pack_bf16(2.f * acc[c][2] - bf_lo(xf[c][1]), 2.f * acc[c][3] - bf_hi(xf[c][1]));
    }
    dense(o, smem, 2, f, L);
#pragma unroll
    for (int c = 0; c < 4; ++c) {
        f[c][0] = pack_bf16(o[c][0], o[c][1]);
        f[c][1] = pack_bf16(o[c][2], o[c][3]);
    }
    lds_wait(lflag + F_DONE, (u32)GPB);
    store_tile(out, node0, nrows, f, ring, lane);
}

extern "C" void kernel_launch(void* const* d_in, const int* in_sizes, int n_in, void* d_out, int out_size, void* d_ws,
                              size_t ws_size, hipStream_t stream) {
    k_cheb<<<NBK, 1024, 0, stream>>>((const u32*)d_in[1], (const u32*)d_in[2], (const u16*)d_in[3],
                                     (const char*)d_in[0], (const char*)d_in[4], (char*)d_ws, (char*)d_out);
}
```

```cpp
#include <hip/hip_runtime.h>
#include <hip/hip_bf16.h>
#include <stdint.h>

typedef unsigned int u32;
typedef unsigned short u16;
typedef unsigned char u8;
typedef u32 u32x4 __attribute__((ext_vector_type(4)));
typedef u32 u32x2 __attribute__((ext_vector_type(2)));
typedef short s16x2 __attribute__((ext_vector_type(2)));
typedef short s16x4 __attribute__((ext_vector_type(4)));
typedef __bf16 bf16x8 __attribute__((ext_vector_type(8)));
typedef __bf16 bf16x2 __attribute__((ext_vector_type(2)));
typedef float f32x4 __attribute__((ext_vector_type(4)));
#define LAS __attribute__((address_space(3)))

constexpr int NN = 50000, NE = 800000;
constexpr int NBK = 256;
constexpr int NB1 = 250, CH = 3200;
constexpr int GPB = 13;
constexpr int SLOTS = 24;
constexpr int CAP2 = 192;

#define DELAY 50
constexpr int NSH = 4;
constexpr int NREP = 64;

constexpr int L_LO = 0;
constexpr int L_NUL = L_LO + GPB * 2 * CAP2 * 4;
constexpr int L_DUMP = L_NUL + 32;
constexpr int L_OH = L_DUMP + 32;
constexpr int L_CNT = L_OH + L_OH / 2;
constexpr int L_TAB = L_CNT + 256;
constexpr int L_W = (L_TAB + GPB * 128 + 255) / 256 * 256;
constexpr int L_RING = L_W + 24576;
constexpr int RINGH = GPB * 4096;
constexpr int L_K2 = L_RING + 2 * RINGH;
constexpr int L_STAGE = L_RING;
constexpr int L_CNTA = L_STAGE + 32768;
static_assert(L_K2 <= 163840 && 32768 + 1040 <= RINGH, "lds");
static_assert(L_OH % 32 == 0 && L_CNT % 16 == 0 && L_TAB % 16 == 0, "align");
enum { F_DONE = 59, F_ARR2 = 60, F_W = 61, F_GO2 = 62 };

constexpr size_t W_LINE = 0;
constexpr size_t W_OVF = W_LINE + (size_t)NBK * NB1 * 128;
constexpr size_t W_HDR = W_OVF + (size_t)NB1 * CH * 16;
constexpr size_t W_BAR = W_HDR + 2048;
constexpr size_t W_T1 = W_BAR + 512 * 256;
static_assert(W_OVF % 128 == 0 && W_BAR % 256 == 0 && W_T1 % 128 == 0 && NREP * NSH == 256, "align");
constexpr u32 HKEY = 0x9e3779b9u;
constexpr unsigned long long TMO = 20000000ull;

__device__ __forceinline__ u32 lds_addr(const void* p) { return (u32)(uintptr_t)(const LAS char*)p; }
__device__ __forceinline__ u32 bstart(u32 b) { return (b * 3125u) >> 4; }
__device__ __forceinline__ u32 bucket_of(u32 d) { return __umulhi(d * 16u + 15u, 1374390u); }

__device__ __forceinline__ u32x4 make_rsrc(const void* base, u32 bytes) {
    const uint64_t a = (uint64_t)base;
    u32x4 r;
    r[0] = (u32)a;
    r[1] = (u32)(a >> 32) & 0xffffu;
    r[2] = bytes;
    r[3] = 0x00020000u;
    return r;
}

template <bool SC1>
__device__ __forceinline__ void dma4(u32x4 rsrc, u32 lds, u32 o0, u32 o1, u32 o2, u32 o3) {
    u32 keep;
    if (SC1)
        asm volatile(
            "s_waitcnt lgkmcnt(0)\n\t"
            "s_mov_b32 %0, m0\n\t"
            "s_mov_b32 m0, %1\n\t"
            "s_nop 0\n\t"
            "buffer_load_dwordx4 %2, %6, 0 offen sc1 lds\n\t"
            "buffer_load_dwordx4 %3, %6, 0 offen offset:1024 sc1 lds\n\t"
            "buffer_load_dwordx4 %4, %6, 0 offen offset:2048 sc1 lds\n\t"
            "buffer_load_dwordx4 %5, %6, 0 offen offset:3072 sc1 lds\n\t"
            "s_mov_b32 m0, %0"
            : "=&s"(keep)
            : "s"(lds), "v"(o0), "v"(o1), "v"(o2), "v"(o3), "s"(rsrc)
            : "memory");
    else
        asm volatile(
            "s_waitcnt lgkmcnt(0)\n\t"
            "s_mov_b32 %0, m0\n\t"
            "s_mov_b32 m0, %1\n\t"
            "s_nop 0\n\t"
            "buffer_load_dwordx4 %2, %6, 0 offen lds\n\t"
            "buffer_load_dwordx4 %3, %6, 0 offen offset:1024 lds\n\t"
            "buffer_load_dwordx4 %4, %6, 0 offen offset:2048 lds\n\t"
            "buffer_load_dwordx4 %5, %6, 0 offen offset:3072 lds\n\t"
            "s_mov_b32 m0, %0"
            : "=&s"(keep)
            : "s"(lds), "v"(o0), "v"(o1), "v"(o2), "v"(o3), "s"(rsrc)
            : "memory");
}
__device__ __forceinline__ void dma2(u32x4 rsrc, u32 lds, u32 o0, u32 o1) {
    u32 keep;
    asm volatile(
        "s_waitcnt lgkmcnt(0)\n\t"
        "s_mov_b32 %0, m0\n\t"
        "s_mov_b32 m0, %1\n\t"
        "s_nop 0\n\t"
        "buffer_load_dwordx4 %2, %4, 0 offen lds\n\t"
        "buffer_load_dwordx4 %3, %4, 0 offen offset:1024 lds\n\t"
        "s_mov_b32 m0, %0"
        : "=&s"(keep)
        : "s"(lds), "v"(o0), "v"(o1), "s"(rsrc)
        : "memory");
}
#define WAIT_VM(n) asm volatile("s_waitcnt vmcnt(" #n ")" ::: "memory")
#define RLX_AGENT __ATOMIC_RELAXED, __HIP_MEMORY_SCOPE_AGENT
#define RLX_WG __ATOMIC_RELAXED, __HIP_MEMORY_SCOPE_WORKGROUP
__device__ __forceinline__ u32x4 ld16(const void* base, u32 bytes, u32 off) {
    return __builtin_amdgcn_raw_buffer_load_b128(__builtin_amdgcn_make_buffer_rsrc((void*)base, 0, bytes, 0x00020000), off, 0, 16);
}
__device__ __forceinline__ void st16(void* p, u32x4 v) {
    asm volatile("global_store_dwordx4 %0, %1, off sc1\n\ts_nop 1" ::"v"(p), "v"(v) : "memory");
}

#define STAMP_DECL do {} while (0)
#define STAMP(k, i) do {} while (0)
#define STAMP_FLUSH(k) do {} while (0)

__device__ __forceinline__ u32 pack_bf16(float a, float b) {
    bf16x2 v;
    v[0] = (__bf16)a;
    v[1] = (__bf16)b;
    return __builtin_bit_cast(u32, v);
}
__device__ __forceinline__ float bf_lo(u32 v) { return __uint_as_float(v << 16); }
__device__ __forceinline__ float bf_hi(u32 v) { return __uint_as_float(v & 0xffff0000u); }

struct Lane {
    u32 coff[4];
    u32 tra[4];
    u32 sh;
    int lane;
};
__device__ __forceinline__ Lane make_lane(int lane) {
    Lane L;
    L.lane = lane;
    const int a = lane >> 3, p = lane & 7;
#pragma unroll
    for (int i = 0; i < 4; ++i) L.coff[i] = (u32)(((p ^ ((((a >> 1) & 1) << 1) | ((i & 1) << 2))) << 4) + 4096 - 1024 * i);
    const int g = lane >> 4, q = (lane & 15) >> 2, pp = lane & 3;
    const int sw = ((g & 1) << 1) | ((q & 1) << 2);
#pragma unroll
    for (int c = 0; c < 4; ++c)
        L.tra[c] = (u32)(128 * (8 * (q & 1) + 2 * g + (q >> 1)) + 16 * ((2 * c + (pp >> 1)) ^ sw) + 8 * (pp & 1));
    L.sh = (u32)(15 - (lane & 15)) * 0x10001u;
    return L;
}
__device__ __forceinline__ bf16x8 afrag(const char* buf, const Lane& L, int c) {
    const s16x4 a0 = __builtin_amdgcn_ds_read_tr16_b64_v4i16((LAS s16x4*)(buf + L.tra[c]));
    const s16x4 a1 = __builtin_amdgcn_ds_read_tr16_b64_v4i16((LAS s16x4*)(buf + L.tra[c] + 2048));
    return __builtin_bit_cast(bf16x8, __builtin_shufflevector(a0, a1, 0, 1, 2, 3, 4, 5, 6, 7));
}
__device__ __forceinline__ u32 row_off(u32 lo, u32 add) {
    u32 r;
    asm("v_mad_u32_u16 %0, %1, %2, %3" : "=v"(r) : "v"(lo), "s"(128u), "v"(add));
    return r;
}
__device__ __forceinline__ u32 sel2(u32 oh, u32 lo0, u32 lo1, u32 sh) {
    const s16x2 m = (__builtin_bit_cast(s16x2, oh) << __builtin_bit_cast(s16x2, sh)) >> 15;
    return __builtin_amdgcn_perm(lo1, lo0, 0x07060302u) & __builtin_bit_cast(u32, m);
}
__device__ __forceinline__ int make_tab(char* smem, int wv, int lane) {
    const u32x4 cv = *(const u32x4*)(smem + L_CNT + 16 + wv * 16);
    const u32 c0 = __builtin_amdgcn_readfirstlane(cv[0]), c1 = __builtin_amdgcn_readfirstlane(cv[1]);
    const u32 c2 = __builtin_amdgcn_readfirstlane(cv[2]), c3 = __builtin_amdgcn_readfirstlane(cv[3]);
    const u32 b0 = (u32)wv * 48u;
    const u32 n0 = (c0 + 31u) >> 5, s1 = max((c1 + 4u) >> 5, n0);
    const u32 e2 = (c2 + 31u) >> 5, s3 = max((c3 + 4u) >> 5, e2);
    const u32 ea = n0 - b0, eb = ea + (e2 - s1), nb = eb + (b0 + 48u - s3), v = (u32)lane;
    u32 pb = v < eb ? s1 - ea : s3 - eb;
    pb = v < ea ? b0 : pb;
    const u32 a = v < nb ? (pb + v) * 32u : (u32)L_NUL;
    *(u16*)(smem + L_TAB + wv * 128 + lane * 2) = (u16)a;
    __builtin_amdgcn_wave_barrier();
    return (int)((nb + 3u) >> 2);
}
struct Blk { u32x4 ix, l0, l1, oh; };
__device__ __forceinline__ Blk read_blk(const char* smem, u32 a, const Lane& L) {
    Blk B;
    B.ix = *(const u32x4*)(smem + a + (L.lane & 8) * 2);
    B.l0 = *(const u32x4*)(smem + a);
    B.l1 = *(const u32x4*)(smem + a + 16);
    B.oh = *(const u32x4*)(smem + L_OH + (a >> 1));
    return B;
}
template <bool SC1>
__device__ __forceinline__ void issue_blk(u32x4 rsrc, const Blk& B, u32 buf, const Lane& L) {
    dma4<SC1>(rsrc, buf, row_off(B.ix[0], L.coff[0]), row_off(B.ix[1], L.coff[1]), row_off(B.ix[2], L.coff[2]), row_off(B.ix[3], L.coff[3]));
}
__device__ __forceinline__ u32x4 sel_blk(const Blk& B, const Lane& L) {
    u32x4 b;
    b[0] = sel2(B.oh[0], B.l0[0], B.l0[1], L.sh);
    b[1] = sel2(B.oh[2], B.l1[0], B.l1[1], L.sh);
    b[2] = sel2(B.oh[1], B.l0[2], B.l0[3], L.sh);
    b[3] = sel2(B.oh[3], B.l1[2], B.l1[3], L.sh);
    return b;
}
__device__ __forceinline__ void own_rows(u32x4 rsrc, u32 buf, int node0, int lane) {
    const u32 o = (u32)(node0 + (lane >> 3)) * 128u + (u32)(((lane & 7) ^ (lane >> 4)) << 4) + 4096u;
    dma2(rsrc, buf, o, o ^ 64u);
}
struct Prime { u32x4 off[2], bq[2]; u32 an; };
__device__ __forceinline__ Prime make_prime(const char* smem, int wv, const Lane& L) {
    const u16* tp = (const u16*)(smem + L_TAB + wv * 128) + (L.lane >> 4);
    Prime P;
#pragma unroll
    for (int h = 0; h < 2; ++h) {
        const Blk B = read_blk(smem, tp[4 * h], L);
#pragma unroll
        for (int i = 0; i < 4; ++i) P.off[h][i] = row_off(B.ix[i], L.coff[i]);
        P.bq[h] = sel_blk(B, L);
    }
    P.an = tp[8];
    return P;
}
template <bool SC1, bool OWN>
__device__ __forceinline__ void gather_group(f32x4 (&acc)[4], u32x4 rsrc, char* smem, int wv, int ns, const Lane& L, const Prime& P,
                                             int node0) {
    char* ring = smem + L_RING + wv * 4096;
    const u32 rb = lds_addr(ring);
    const u16* tp = (const u16*)(smem + L_TAB + wv * 128) + (L.lane >> 4);
    u32x4 bq[2] = {P.bq[0], P.bq[1]};
    __builtin_amdgcn_s_setprio(3);
#pragma unroll
    for (int h = 0; h < 2; ++h)
        if (h < ns) dma4<SC1>(rsrc, rb + h * RINGH, P.off[h][0], P.off[h][1], P.off[h][2], P.off[h][3]);
        else if (OWN && h == ns) own_rows(rsrc, rb + h * RINGH, node0, L.lane);
    u32 an = P.an;
    int rem = ns;
    while (rem > 0) {
#pragma unroll
        for (int h = 0; h < 2; ++h) {
            if (h == 1 && rem == 0) break;
            if (rem == 7) __builtin_amdgcn_s_setprio(2);
            else if (rem == 5) __builtin_amdgcn_s_setprio(1);
            else if (rem == 3) __builtin_amdgcn_s_setprio(0);
            const Blk B = read_blk(smem, an, L);
            an = tp[4 * (h + 3)];
            if (rem > 1) WAIT_VM(4); else if (OWN) WAIT_VM(2); else WAIT_VM(0);
            bf16x8 a[4];
#pragma unroll
            for (int c = 0; c < 4; ++c) a[c] = afrag(ring + h * RINGH, L, c);
            if (rem > 2) issue_blk<SC1>(rsrc, B, rb + h * RINGH, L);
            else if (OWN && rem == 2) own_rows(rsrc, rb + h * RINGH, node0, L.lane);
#pragma unroll
            for (int c = 0; c < 4; ++c)
                acc[c] = __builtin_amdgcn_mfma_f32_16x16x32_bf16(a[c], __builtin_bit_cast(bf16x8, bq[h]), acc[c], 0, 0, 0);
            bq[h] = sel_blk(B, L);
            --rem;
        }
        tp += 8;
    }
    __builtin_amdgcn_s_setprio(0);
}

__device__ __forceinline__ void put_record(char* smem, u32 lo, u32 meta) {
    const u32 q = ((lo & 0xffffu) * 1342u) >> 24;
    u32 a = atomicAdd((u32*)(smem + L_CNT + (meta & 0xf0u) + q * 4u), (q & 1u) ? (u32)-4 : 4u);
    a = min(a, (u32)L_DUMP);
    *(u32*)(smem + a) = lo;
    *(u16*)(smem + L_OH + (a >> 1)) = (u16)(1u << (meta & 15u));
}
struct Pos { u32 a[3], oh[3]; bool ok[3]; };
__device__ __forceinline__ Pos claim_chunk(char* smem, u32x4 v) {
    Pos P;
#pragma unroll
    for (int k = 0; k < 3; ++k) {
        const u32 g = (v[3] >> (8 * k)) & 0xf0u;
        const u32 m = (v[k] & 0xffffu) * 1342u;
        u32 dir;
        asm("v_bfe_i32 %0, %1, 24, 1\n\tv_lshl_or_b32 %0, %0, 3, 4" : "=v"(dir) : "v"(m));
        if (k == 0) asm("v_lshlrev_b16_sdwa %0, %1, %2 dst_sel:DWORD dst_unused:UNUSED_PAD src0_sel:BYTE_0 src1_sel:DWORD" : "=v"(P.oh[k]) : "v"(v[3]), "v"(1u));
        if (k == 1) asm("v_lshlrev_b16_sdwa %0, %1, %2 dst_sel:DWORD dst_unused:UNUSED_PAD src0_sel:BYTE_1 src1_sel:DWORD" : "=v"(P.oh[k]) : "v"(v[3]), "v"(1u));
        if (k == 2) asm("v_lshlrev_b16_sdwa %0, %1, %2 dst_sel:DWORD dst_unused:UNUSED_PAD src0_sel:BYTE_2 src1_sel:DWORD" : "=v"(P.oh[k]) : "v"(v[3]), "v"(1u));
        P.ok[k] = g != 0u;
        P.a[k] = dir;
        if (P.ok[k]) P.a[k] = atomicAdd((u32*)(smem + L_CNT + (g | ((m >> 22) & 0xcu))), dir);
    }
    return P;
}
__device__ __forceinline__ void write_chunk(char* smem, Pos& P, u32x4 v) {
    asm volatile("" : "+v"(P.a[0]), "+v"(P.a[1]), "+v"(P.a[2]));
#pragma unroll
    for (int k = 0; k < 3; ++k) {
        const u32 p = min(P.a[k], (u32)L_DUMP);
        if (P.ok[k]) {
            *(u32*)(smem + p) = v[k];
            *(u16*)(smem + L_OH + (p >> 1)) = (u16)P.oh[k];
        }
    }
}
__device__ __forceinline__ void put_surplus(char* smem, u32x4 r, u32 bucket) {
    if (bucket_of(r[1]) == bucket) put_record(smem, r[0], r[1] - bstart(bucket) + 16u);
}
__device__ __forceinline__ u32 wait_hdr(const unsigned long long* hdr, int b, u32 epoch) {
    const unsigned long long t0 = __builtin_amdgcn_s_memrealtime();
    for (;;) {
        const unsigned long long h = __hip_atomic_load(hdr + b, RLX_AGENT);
        if (__builtin_amdgcn_readfirstlane((u32)(h >> 32)) == (epoch ^ HKEY)) return min((u32)__builtin_amdgcn_readfirstlane((u32)h), (u32)CH);
        if (__builtin_amdgcn_s_memrealtime() - t0 > TMO) return 0u;
        __builtin_amdgcn_s_sleep(2);
    }
}
template <bool FIRST>
__device__ __forceinline__ void init_lists(char* smem, int t) {
#pragma unroll
    for (int h = 0; h < 2; ++h) {
        const int i = t + h * 1024;
        if (i < L_CNT / 16) *(u32x4*)(smem + i * 16) = (u32x4)(i < L_OH / 16 ? 0xffffffffu : 0u);
    }
    if (t >= 960) {
        const u32 b = (u32)t - 960u - 4u;
        if (b < 4u * GPB) *(u32*)(smem + L_CNT + 16 + b * 4) = (b >> 1) * (CAP2 * 4u) + ((b & 1u) ? (CAP2 - 1) * 4u : 0u);
        else if (FIRST) *(u32*)(smem + L_CNT + 16 + (int)b * 4) = 0u;
    }
}

template <bool SC1>
__device__ __forceinline__ void slow_bucket(char* smem, f32x4 (&acc)[4], const char* bl, const char* ovf,
                                            const unsigned long long* hdr, u32 epoch, u32 bucket,
                                            u32x4 rsrc, int t, int wv, const Lane& L) {
    int blk = -((NB1 + 7) / 8);
    u32 o0 = 0, n = 0;
    for (;;) {
        if (blk >= 0 && o0 >= n) {
            for (; blk < NB1; ++blk)
                if (__any(__hip_atomic_load((const u32*)(bl + blk * 128 + (t & 7) * 16 + 12), RLX_AGENT) >> 31)) break;
            if (blk >= NB1) break;
            n = wait_hdr(hdr, blk, epoch);
            o0 = 0;
        }
        __syncthreads();
        init_lists<false>(smem, t);
        __syncthreads();
        if (blk < 0) {
            const int c = (blk + (NB1 + 7) / 8) * 64 + t;
            if (t < 64 && c < NB1 * 8) { const u32x4 v = ld16(bl, NB1 * 128, c * 16); Pos P = claim_chunk(smem, v); write_chunk(smem, P, v); }
            ++blk;
        } else {
            if (t < CAP2 && o0 + t < n) put_surplus(smem, ld16(ovf, NB1 * CH * 16, (blk * CH + o0 + t) * 16), bucket);
            o0 += CAP2;
            if (o0 >= n) ++blk;
        }
        __syncthreads();
        if (wv < GPB) {
            const int ns = make_tab(smem, wv, L.lane);
            gather_group<SC1, false>(acc, rsrc, smem, wv, ns, L, make_prime(smem, wv, L), 0);
        }
    }
}

__device__ __forceinline__ void store_tile(char* __restrict__ outp, int node0, int nrows, const u32x2 (&v)[4], char* buf, int lane) {
    const int n = lane & 15, q = lane >> 4;
#pragma unroll
    for (int c = 0; c < 4; ++c) *(u32x2*)(buf + n * 128 + (((2 * c + (q >> 1)) ^ (n & 7)) << 4) + 8 * (q & 1)) = v[c];
    __builtin_amdgcn_wave_barrier();
    char* p = outp + (size_t)node0 * 128 + lane * 16;
    const int r = lane >> 3;
#pragma unroll
    for (int h = 0; h < 2; ++h)
        if (r + 8 * h < nrows) st16(p + h * 1024, *(const u32x4*)(buf + h * 1024 + r * 128 + (((lane & 7) ^ r) << 4)));
}
__device__ __forceinline__ void dense(f32x4 (&o)[4], const char* smem, int k, const u32x2 (&f)[4], const Lane& L) {
#pragma unroll
    for (int kk = 0; kk < 2; ++kk) {
        u32x4 b;
        b[0] = f[2 * kk][0]; b[1] = f[2 * kk][1]; b[2] = f[2 * kk + 1][0]; b[3] = f[2 * kk + 1][1];
#pragma unroll
        for (int c = 0; c < 4; ++c)
            o[c] = __builtin_amdgcn_mfma_f32_16x16x32_bf16(afrag(smem + L_W + (2 * k + kk) * 4096, L, c), __builtin_bit_cast(bf16x8, b), o[c], 0, 0, 0);
    }
}

__device__ __forceinline__ void bar_arrive(u32* bar, int lane) {
    if (lane < NREP) __hip_atomic_fetch_add(bar + (lane * NSH + blockIdx.x % NSH) * 64, 1u, RLX_AGENT);
}
__device__ __forceinline__ void bar_wait(const u32* bar, int lane) {
    const u32* pb = bar + ((blockIdx.x / NSH) % NREP * NSH + (lane % NSH)) * 64;
    const unsigned long long t0 = __builtin_amdgcn_s_memrealtime();
    u32 c = lane < NSH ? 0u : (u32)(NBK / NSH);
    while (__builtin_amdgcn_s_memrealtime() - t0 < TMO) {
        if (c != (u32)(NBK / NSH)) c = __hip_atomic_load(pb, RLX_AGENT);
        if (__all(c == (u32)(NBK / NSH))) break;
        __builtin_amdgcn_s_sleep(2);
    }
}
__device__ __forceinline__ void lds_wait(const u32* f, u32 n) {
    while (__hip_atomic_load(f, RLX_WG) < n) __builtin_amdgcn_s_sleep(1);
    asm volatile("" ::: "memory");
}

__device__ __forceinline__ void wait_lines(u32x4& v, const char* bl, u32 off, u32 tag, int lane) {
    const bool in = off < (u32)(NB1 * 128);
    unsigned long long bad = __ballot(in && ((v[3] >> 24) & 0x7fu) != tag);
    if (bad != 0ull) {
        const unsigned long long t0 = __builtin_amdgcn_s_memrealtime();
        do {
            __builtin_amdgcn_s_sleep(2);
            asm volatile("" ::: "memory");
            if ((bad >> (lane & 56)) & 0xffull) v = ld16(bl, NB1 * 128, off);
            asm volatile("" : "+v"(v));
            bad = __ballot(in && ((v[3] >> 24) & 0x7fu) != tag);
        } while (bad != 0ull && __builtin_amdgcn_s_memrealtime() - t0 < TMO);
        if (bad != 0ull) v[3] = 0u;
    }
}
__device__ __forceinline__ u32x4 first_surplus(unsigned long long f, int b0, const char* ovf, int lane) {
    return ld16(ovf, NB1 * CH * 16, (u32)((b0 + (__builtin_ctzll(f) >> 3)) * CH + lane) * 16u);
}
__device__ __forceinline__ void take_surplus(char* smem, unsigned long long f, int b0, u32x4 r, const char* ovf,
                                             const unsigned long long* hdr, u32 epoch, u32 bucket, int lane) {
    for (;;) {
        const int j = __builtin_ctzll(f) >> 3, b = b0 + j;
        const u32 m = (u32)(f >> (8 * j)) & 0xffu;
        bool done = false;
        for (int it = 0; m < 64u; ++it) {
            const bool mine = r[2] == epoch && r[3] == ~epoch && bucket_of(r[1] & 0xffffu) == bucket;
            if ((u32)__builtin_popcountll(__ballot(mine)) == m) {
                if (mine) put_surplus(smem, r, bucket);
                done = true;
                break;
            }
            if (it == 3) break;
            asm volatile("" ::: "memory");
            r = first_surplus(f, b0, ovf, lane);
        }
        if (!done) {
            const u32 n = wait_hdr(hdr, b, epoch);
            for (u32 o = lane; o < n; o += 64) put_surplus(smem, ld16(ovf, NB1 * CH * 16, (b * CH + o) * 16u), bucket);
        }
        f &= ~(0xffull << (8 * j));
        if (f == 0ull) break;
        r = first_surplus(f, b0, ovf, lane);
    }
}

__global__ __launch_bounds__(1024, 4) void k_cheb(const u32* __restrict__ src, const u32* __restrict__ dst,
                                               const u16* __restrict__ ewp, const char* __restrict__ x,
                                               const char* __restrict__ w, char* ws, char* __restrict__ out) {
    __shared__ __attribute__((aligned(256))) char smem[L_K2];
    char* lines = ws + W_LINE;
    char* ovf = ws + W_OVF;
    unsigned long long* hdr = (unsigned long long*)(ws + W_HDR);
    u32* bar = (u32*)(ws + W_BAR);
    char* t1 = ws + W_T1;
    const int t = threadIdx.x, lane = t & 63, wv = __builtin_amdgcn_readfirstlane(t >> 6);
    const int blk = blockIdx.x;
    const int xr = blk >> 3;
    const int bucket = (blk & 7) * 32 + xr;
    const bool active = wv < GPB;
    const int node0 = (int)bstart(bucket) + wv * 16;
    const int nrows = min((int)bstart(bucket + 1) - node0, 16);
    char* ring = smem + L_RING + wv * 4096;
    u32* lflag = (u32*)(smem + L_CNT);
    Lane L = make_lane(lane);
    STAMP_DECL;
    STAMP(0, 0);
    u32 epoch;
    {
        u32* stage = (u32*)(smem + L_STAGE);
        u32* cnt = (u32*)(smem + L_CNTA);
        const u32 nb = blk < NB1 ? (u32)CH : 0u;
        const size_t e0i = (size_t)min(blk, NB1 - 1) * CH;
        const u32x4 d = __builtin_amdgcn_raw_buffer_load_b128(__builtin_amdgcn_make_buffer_rsrc((void*)(dst + e0i), 0, nb * 4u, 0x00020000), t * 16, 0, 2);
        const u32 e0 = __hip_atomic_load(bar + 288 * 64, RLX_AGENT);
        const u32x4 s = __builtin_amdgcn_raw_buffer_load_b128(__builtin_amdgcn_make_buffer_rsrc((void*)(src + e0i), 0, nb * 4u, 0x00020000), t * 16, 0, 2);
        const u32x2 ew = __builtin_amdgcn_raw_buffer_load_b64(__builtin_amdgcn_make_buffer_rsrc((void*)(ewp + e0i), 0, nb * 2u, 0x00020000), t * 8, 0, 2);
        if (blk == NBK - 1 && t < 256) __hip_atomic_store(bar + t * 64, 0u, RLX_AGENT);
        if (t < 512) *(u32x4*)&stage[3 * 2048 + t * 4] = (u32x4)0u;
        else if (t < 512 + 65) *(u32x4*)&cnt[(t - 512) * 4] = (u32x4)0u;
        init_lists<true>(smem, t);
        asm volatile("" : "+v"(L.coff[0]), "+v"(L.coff[1]), "+v"(L.coff[2]), "+v"(L.coff[3]), "+v"(L.tra[0]), "+v"(L.tra[1]),
                     "+v"(L.tra[2]), "+v"(L.tra[3]), "+v"(L.sh));
        __syncthreads();
        __builtin_amdgcn_sched_barrier(0);
        const bool bin = t < CH / 4 && blk < NB1;
        u32 rk[4], bk[4];
#pragma unroll
        for (int i = 0; i < 4; ++i) {
            bk[i] = bucket_of((u32)d[i]);
            rk[i] = 0u;
            if (bin) rk[i] = atomicAdd(&cnt[bk[i]], 1u);
        }
        asm volatile("" :: "v"(s), "v"(ew));
        epoch = __builtin_amdgcn_readfirstlane(e0) + 1u;
        if (bin) {
#pragma unroll
            for (int i = 0; i < 4; ++i) {
                const u32 lo = (u32)s[i] | ((i & 1) ? (ew[i >> 1] & 0xffff0000u) : (ew[i >> 1] << 16));
                const u32 c = bk[i] * 8u + (rk[i] & 7u), sub = rk[i] >> 3;
                if (__builtin_expect(rk[i] < (u32)SLOTS, 1)) {
                    stage[sub * 2048u + c] = lo;
                    ((u8*)&stage[3 * 2048 + c])[sub] = (u8)((u32)d[i] - bstart(bk[i]) + 16u);
                } else {
                    const u32 o = atomicAdd(&cnt[256], 1u);
                    u32x4 r;
                    r[0] = lo; r[1] = (u32)d[i]; r[2] = epoch; r[3] = ~epoch;
                    st16(ovf + (size_t)(blk * CH + o) * 16, r);
                }
            }
        }
        if (blk == NBK - 1) WAIT_VM(0);
        STAMP(0, 1);
        __syncthreads();
        if (blk < NB1) {
#pragma unroll
            for (int h = 0; h < 2; ++h) {
                const int c = t + h * 1024;
                const u32 m = min(max(cnt[c >> 3], (u32)SLOTS) - (u32)SLOTS, 255u);
                u32x4 v;
                v[0] = stage[c]; v[1] = stage[2048 + c]; v[2] = stage[4096 + c];
                v[3] = stage[6144 + c] | ((epoch & 0x7fu) << 24) | ((m >> (c & 7)) << 31);
                st16(lines + ((size_t)(c >> 3) * NB1 + blk) * 128 + (c & 7) * 16, v);
            }
        } else if (blk == NBK - 1 && t < 32) {
            __hip_atomic_store((unsigned long long*)(bar + (256 + t) * 64), (unsigned long long)epoch | ((unsigned long long)(epoch ^ HKEY) << 32), RLX_AGENT);
        }
    }
    STAMP(0, 2);
    const char* bl = lines + (size_t)bucket * (NB1 * 128);
    {
        __builtin_amdgcn_s_sleep(DELAY);
        if (blk >= NB1) __builtin_amdgcn_s_sleep(30);
        u32x4 v0 = ld16(bl, NB1 * 128, t * 16);
        u32x4 v1 = ld16(bl, NB1 * 128, (t + 1024) * 16);
        u32x4 r0 = 0u, r1 = 0u;
        wait_lines(v0, bl, t * 16, epoch & 0x7fu, lane);
        const unsigned long long f0 = __ballot(v0[3] >> 31);
        if (__builtin_expect(f0 != 0ull, 0)) r0 = first_surplus(f0, wv * 8, ovf, lane);
        {
            Pos P0 = claim_chunk(smem, v0);
            write_chunk(smem, P0, v0);
        }
        __builtin_amdgcn_sched_barrier(0);
        wait_lines(v1, bl, (t + 1024) * 16, epoch & 0x7fu, lane);
        STAMP(0, 3);
        const unsigned long long f1 = __ballot(v1[3] >> 31);
        if (__builtin_expect(f1 != 0ull, 0)) r1 = first_surplus(f1, wv * 8 + 128, ovf, lane);
        {
            Pos P1 = claim_chunk(smem, v1);
            write_chunk(smem, P1, v1);
        }
        {
            u32* cnt = (u32*)(smem + L_CNTA);
            const u32 novf = cnt[256];
            if (__builtin_expect(novf != 0u, 0)) {
                WAIT_VM(0);
                u32 old = 0;
                if (lane == 0) old = atomicAdd(&cnt[257], 1u);
                if (old == 15u) __hip_atomic_store(hdr + blk, (unsigned long long)novf | ((unsigned long long)(epoch ^ HKEY) << 32), RLX_AGENT);
            }
        }
        if (__builtin_expect(f0 != 0ull, 0)) take_surplus(smem, f0, wv * 8, r0, ovf, hdr, epoch, bucket, lane);
        if (__builtin_expect(f1 != 0ull, 0)) take_surplus(smem, f1, wv * 8 + 128, r1, ovf, hdr, epoch, bucket, lane);
    }
    __syncthreads();
    STAMP(0, 4);
    if (!active) {
        __builtin_amdgcn_s_setprio(3);
        const u32x4 wrs = make_rsrc(w - 4096, 3u * 64u * 128u + 4096u);
        const int a = lane >> 3;
#pragma unroll
        for (int h = 0; h < 2; ++h) {
            const int ks = (wv - GPB) * 2 + h;
            const u32 rb = (u32)(32 * ks + 4 * (a >> 1) + 2 * (a & 1)) * 128u;
            dma4<false>(wrs, lds_addr(smem + L_W + ks * 4096), rb + L.coff[0], rb + 128u + L.coff[1], rb + 2048u + L.coff[2], rb + 2176u + L.coff[3]);
        }
    }
    bool slow;
    {
        const u32x2 c = *(const u32x2*)(smem + L_CNT + 16 + min(lane, 2 * GPB - 1) * 8);
        slow = __builtin_expect(__any((int)c[0] > (int)c[1] + 4), 0);
    }
    if (!active) WAIT_VM(0);
    if (wv == 15) {
        const unsigned long long* rp = (const unsigned long long*)(bar + (256 + xr) * 64);
        const unsigned long long t0 = __builtin_amdgcn_s_memrealtime();
        while (__hip_atomic_load(rp, RLX_AGENT) != ((unsigned long long)epoch | ((unsigned long long)(epoch ^ HKEY) << 32)) &&
               __builtin_amdgcn_s_memrealtime() - t0 < TMO) __builtin_amdgcn_s_sleep(4);
    }
    if (!active && lane == 0) __hip_atomic_fetch_add(lflag + F_W, 1u, RLX_WG);
    if (!slow && !active) return;
    f32x4 acc[4], o[4];
#pragma unroll
    for (int c = 0; c < 4; ++c) { acc[c] = 0.f; o[c] = 0.f; }
    int ns = 0;
    {
        const u32x4 rsrc = make_rsrc(x - 4096, (u32)NN * 128u + 4096u);
        if (__builtin_expect(!slow, 1)) {
            ns = make_tab(smem, wv, lane);
            gather_group<false, true>(acc, rsrc, smem, wv, ns, L, make_prime(smem, wv, L), node0);
        } else {
            slow_bucket<false>(smem, acc, bl, ovf, hdr, epoch, bucket, rsrc, t, wv, L);
            if (active) own_rows(rsrc, lds_addr(ring), node0, lane);
        }
    }
    STAMP(0, 5);
    Prime P2;
    u32x2 xf[4];
    if (active) {
        u32x2 tf[4];
#pragma unroll
        for (int c = 0; c < 4; ++c) {
            tf[c][0] = pack_bf16(acc[c][0], acc[c][1]);
            tf[c][1] = pack_bf16(acc[c][2], acc[c][3]);
            acc[c] = 0.f;
        }
        store_tile(t1, node0, nrows, tf, ring + (~ns & 1) * RINGH, lane);
        lds_wait(lflag + F_W, 3u);
        dense(o, smem, 1, tf, L);
        WAIT_VM(0);
        u32 old = 0;
        if (lane == 0) old = __hip_atomic_fetch_add(lflag + F_ARR2, 1u, RLX_WG);
        const bool last = __builtin_amdgcn_readfirstlane(old) == (u32)(GPB - 1);
        if (last) bar_arrive(bar, lane);
        __builtin_amdgcn_sched_barrier(0);
        const int n = lane & 15, q = lane >> 4;
#pragma unroll
        for (int c = 0; c < 4; ++c)
            xf[c] = *(const u32x2*)(ring + (ns & 1) * RINGH + n * 128 + (((2 * c + (q >> 1)) ^ (n >> 1)) << 4) + 8 * (q & 1));
        dense(o, smem, 0, xf, L);
        P2 = make_prime(smem, wv, L);
        asm volatile("" : "+v"(P2.off[0]), "+v"(P2.off[1]), "+v"(P2.bq[0]), "+v"(P2.bq[1]), "+v"(P2.an));
        if (last) {
            bar_wait(bar, lane);
            if (lane == 0) __hip_atomic_store(lflag + F_GO2, 1u, RLX_WG);
            if (blk == NBK - 1 && lane == 0) __hip_atomic_store(bar + 288 * 64, epoch, RLX_AGENT);
        }
    }
    lds_wait(lflag + F_GO2, 1u);
    STAMP(0, 6);
    {
        const u32x4 rsrc = make_rsrc(t1 - 4096, (u32)NN * 128u + 4096u);
        if (__builtin_expect(!slow, 1)) gather_group<true, false>(acc, rsrc, smem, wv, ns, L, P2, 0);
        else slow_bucket<true>(smem, acc, bl, ovf, hdr, epoch, bucket, rsrc, t, wv, L);
    }
    if (!active) return;
    u32x2 f[4];
#pragma unroll
    for (int c = 0; c < 4; ++c) {
        f[c][0] = pack_bf16(2.f * acc[c][0] - bf_lo(xf[c][0]), 2.f * acc[c][1] - bf_hi(xf[c][0]));
        f[c][1] = pack_bf16(2.f * acc[c][2] - bf_lo(xf[c][1]), 2.f * acc[c][3] - bf_hi(xf[c][1]));
    }
    dense(o, smem, 2, f, L);
#pragma unroll
    for (int c = 0; c < 4; ++c) {
        f[c][0] = pack_bf16(o[c][0], o[c][1]);
        f[c][1] = pack_bf16(o[c][2], o[c][3]);
    }
    if (lane == 0) __hip_atomic_fetch_add(lflag + F_DONE, 1u, RLX_WG);
    lds_wait(lflag + F_DONE, (u32)GPB);
    store_tile(out, node0, nrows, f, ring, lane);
}

extern "C" void kernel_launch(void* const* d_in, const int* in_sizes, int n_in, void* d_out, int out_size, void* d_ws,
                              size_t ws_size, hipStream_t stream) {
    k_cheb<<<NBK, 1024, 0, stream>>>((const u32*)d_in[1], (const u32*)d_in[2], (const u16*)d_in[3],
                                     (const char*)d_in[0], (const char*)d_in[4], (char*)d_ws, (char*)d_out);
}
```

```cpp
#include <hip/hip_runtime.h>
#include <hip/hip_bf16.h>
#include <stdint.h>

typedef unsigned int u32;
typedef unsigned short u16;
typedef unsigned char u8;
typedef u32 u32x4 __attribute__((ext_vector_type(4)));
typedef u32 u32x2 __attribute__((ext_vector_type(2)));
typedef short s16x2 __attribute__((ext_vector_type(2)));
typedef short s16x4 __attribute__((ext_vector_type(4)));
typedef __bf16 bf16x8 __attribute__((ext_vector_type(8)));
typedef __bf16 bf16x2 __attribute__((ext_vector_type(2)));
typedef float f32x4 __attribute__((ext_vector_type(4)));
#define LAS __attribute__((address_space(3)))

constexpr int NN = 50000;
constexpr int NBK = 256;
constexpr int NB1 = 250, CH = 3200;
constexpr int GPB = 13;
constexpr int SLOTS = 24;
constexpr int CAP2 = 192;

constexpr int DELAY = 50;
constexpr int NSH = 4;
constexpr int NREP = 64;

constexpr int L_LO = 0;
constexpr int L_NUL = L_LO + GPB * 2 * CAP2 * 4;
constexpr int L_DUMP = L_NUL + 32;
constexpr int L_OH = L_DUMP + 32;
constexpr int L_CNT = L_OH + L_OH / 2;
constexpr int L_TAB = L_CNT + 256;
constexpr int L_W = (L_TAB + GPB * 128 + 255) / 256 * 256;
constexpr int L_RING = L_W + 24576;
constexpr int RINGH = GPB * 4096;
constexpr int L_K2 = L_RING + 2 * RINGH;
constexpr int L_STAGE = L_RING;
constexpr int L_CNTA = L_STAGE + 32768;
static_assert(L_K2 <= 163840 && 32768 + 1040 <= RINGH, "lds");
static_assert(L_OH % 32 == 0 && L_CNT % 16 == 0 && L_TAB % 16 == 0, "align");
enum { F_BC = 56, F_ARR2 = 60, F_W = 61, F_GO2 = 62 };

constexpr size_t W_LINE = 0;
constexpr size_t W_OVF = W_LINE + (size_t)NBK * NB1 * 128;
constexpr size_t W_HDR = W_OVF + (size_t)NB1 * CH * 16;
constexpr size_t W_BAR = W_HDR + 2048;
constexpr size_t W_T1 = W_BAR + 512 * 256;
static_assert(W_OVF % 128 == 0 && W_BAR % 256 == 0 && W_T1 % 128 == 0 && NREP * NSH == 256, "align");
constexpr u32 HKEY = 0x9e3779b9u;
constexpr unsigned long long TMO = 20000000ull;

__device__ __forceinline__ u32 lds_addr(const void* p) { return (u32)(uintptr_t)(const LAS char*)p; }
__device__ __forceinline__ u32 bstart(u32 b) { return (b * 3125u) >> 4; }
__device__ __forceinline__ u32 bucket_of(u32 d) { return __umulhi(d * 16u + 15u, 1374390u); }

__device__ __forceinline__ u32x4 make_rsrc(const void* base, u32 bytes) {
    const uint64_t a = (uint64_t)base;
    u32x4 r;
    r[0] = (u32)a;
    r[1] = (u32)(a >> 32) & 0xffffu;
    r[2] = bytes;
    r[3] = 0x00020000u;
    return r;
}

template <bool SC1>
__device__ __forceinline__ void dma4(u32x4 rsrc, u32 lds, u32 o0, u32 o1, u32 o2, u32 o3) {
    u32 keep;
    if (SC1)
        asm volatile(
            "s_waitcnt lgkmcnt(0)\n\t"
            "s_mov_b32 %0, m0\n\t"
            "s_mov_b32 m0, %1\n\t"
            "s_nop 0\n\t"
            "buffer_load_dwordx4 %2, %6, 0 offen sc1 lds\n\t"
            "buffer_load_dwordx4 %3, %6, 0 offen offset:1024 sc1 lds\n\t"
            "buffer_load_dwordx4 %4, %6, 0 offen offset:2048 sc1 lds\n\t"
            "buffer_load_dwordx4 %5, %6, 0 offen offset:3072 sc1 lds\n\t"
            "s_mov_b32 m0, %0"
            : "=&s"(keep)
            : "s"(lds), "v"(o0), "v"(o1), "v"(o2), "v"(o3), "s"(rsrc)
            : "memory");
    else
        asm volatile(
            "s_waitcnt lgkmcnt(0)\n\t"
            "s_mov_b32 %0, m0\n\t"
            "s_mov_b32 m0, %1\n\t"
            "s_nop 0\n\t"
            "buffer_load_dwordx4 %2, %6, 0 offen lds\n\t"
            "buffer_load_dwordx4 %3, %6, 0 offen offset:1024 lds\n\t"
            "buffer_load_dwordx4 %4, %6, 0 offen offset:2048 lds\n\t"
            "buffer_load_dwordx4 %5, %6, 0 offen offset:3072 lds\n\t"
            "s_mov_b32 m0, %0"
            : "=&s"(keep)
            : "s"(lds), "v"(o0), "v"(o1), "v"(o2), "v"(o3), "s"(rsrc)
            : "memory");
}
__device__ __forceinline__ void dma2(u32x4 rsrc, u32 lds, u32 o0, u32 o1) {
    u32 keep;
    asm volatile(
        "s_waitcnt lgkmcnt(0)\n\t"
        "s_mov_b32 %0, m0\n\t"
        "s_mov_b32 m0, %1\n\t"
        "s_nop 0\n\t"
        "buffer_load_dwordx4 %2, %4, 0 offen lds\n\t"
        "buffer_load_dwordx4 %3, %4, 0 offen offset:1024 lds\n\t"
        "s_mov_b32 m0, %0"
        : "=&s"(keep)
        : "s"(lds), "v"(o0), "v"(o1), "s"(rsrc)
        : "memory");
}
#define WAIT_VM(n) asm volatile("s_waitcnt vmcnt(" #n ")" ::: "memory")
#define RLX_AGENT __ATOMIC_RELAXED, __HIP_MEMORY_SCOPE_AGENT
#define RLX_WG __ATOMIC_RELAXED, __HIP_MEMORY_SCOPE_WORKGROUP
__device__ __forceinline__ u32x4 ld16(const void* base, u32 bytes, u32 off) {
    return __builtin_amdgcn_raw_buffer_load_b128(__builtin_amdgcn_make_buffer_rsrc((void*)base, 0, bytes, 0x00020000), off, 0, 16);
}
__device__ __forceinline__ void st16(void* p, u32x4 v) {
    asm volatile("global_store_dwordx4 %0, %1, off sc1\n\ts_nop 1" ::"v"(p), "v"(v) : "memory");
}

__device__ __forceinline__ u32 pack_bf16(float a, float b) {
    bf16x2 v;
    v[0] = (__bf16)a;
    v[1] = (__bf16)b;
    return __builtin_bit_cast(u32, v);
}
__device__ __forceinline__ float bf_lo(u32 v) { return __uint_as_float(v << 16); }
__device__ __forceinline__ float bf_hi(u32 v) { return __uint_as_float(v & 0xffff0000u); }

struct Lane {
    u32 coff[4];
    u32 tra[4];
    u32 sh;
    int lane;
};
__device__ __forceinline__ Lane make_lane(int lane) {
    Lane L;
    L.lane = lane;
    const int a = lane >> 3, p = lane & 7;
#pragma unroll
    for (int i = 0; i < 4; ++i) L.coff[i] = (u32)(((p ^ ((((a >> 1) & 1) << 1) | ((i & 1) << 2))) << 4) + 4096 - 1024 * i);
    const int g = lane >> 4, q = (lane & 15) >> 2, pp = lane & 3;
    const int sw = ((g & 1) << 1) | ((q & 1) << 2);
#pragma unroll
    for (int c = 0; c < 4; ++c)
        L.tra[c] = (u32)(128 * (8 * (q & 1) + 2 * g + (q >> 1)) + 16 * ((2 * c + (pp >> 1)) ^ sw) + 8 * (pp & 1));
    L.sh = (u32)(15 - (lane & 15)) * 0x10001u;
    return L;
}
__device__ __forceinline__ bf16x8 afrag(const char* buf, const Lane& L, int c) {
    const s16x4 a0 = __builtin_amdgcn_ds_read_tr16_b64_v4i16((LAS s16x4*)(buf + L.tra[c]));
    const s16x4 a1 = __builtin_amdgcn_ds_read_tr16_b64_v4i16((LAS s16x4*)(buf + L.tra[c] + 2048));
    return __builtin_bit_cast(bf16x8, __builtin_shufflevector(a0, a1, 0, 1, 2, 3, 4, 5, 6, 7));
}
__device__ __forceinline__ u32 row_off(u32 lo, u32 add) {
    u32 r;
    asm("v_mad_u32_u16 %0, %1, %2, %3" : "=v"(r) : "v"(lo), "s"(128u), "v"(add));
    return r;
}
__device__ __forceinline__ u32 sel2(u32 oh, u32 lo0, u32 lo1, u32 sh) {
    const s16x2 m = (__builtin_bit_cast(s16x2, oh) << __builtin_bit_cast(s16x2, sh)) >> 15;
    return __builtin_amdgcn_perm(lo1, lo0, 0x07060302u) & __builtin_bit_cast(u32, m);
}
__device__ __forceinline__ int make_tab(char* smem, int wv, int lane) {
    const u32x4 cv = *(const u32x4*)(smem + L_CNT + 16 + wv * 16);
    const u32 c0 = __builtin_amdgcn_readfirstlane(cv[0]), c1 = __builtin_amdgcn_readfirstlane(cv[1]);
    const u32 c2 = __builtin_amdgcn_readfirstlane(cv[2]), c3 = __builtin_amdgcn_readfirstlane(cv[3]);
    const u32 b0 = (u32)wv * 48u;
    const u32 n0 = (c0 + 31u) >> 5, s1 = max((c1 + 4u) >> 5, n0);
    const u32 e2 = (c2 + 31u) >> 5, s3 = max((c3 + 4u) >> 5, e2);
    const u32 ea = n0 - b0, eb = ea + (e2 - s1), nb = eb + (b0 + 48u - s3), v = (u32)lane;
    u32 pb = v < eb ? s1 - ea : s3 - eb;
    pb = v < ea ? b0 : pb;
    const u32 a = v < nb ? (pb + v) * 32u : (u32)L_NUL;
    *(u16*)(smem + L_TAB + wv * 128 + lane * 2) = (u16)a;
    __builtin_amdgcn_wave_barrier();
    return (int)((nb + 3u) >> 2);
}
struct Blk { u32x4 ix, l0, l1, oh; };
__device__ __forceinline__ Blk read_blk(const char* smem, u32 a, const Lane& L) {
    Blk B;
    B.ix = *(const u32x4*)(smem + a + (L.lane & 8) * 2);
    B.l0 = *(const u32x4*)(smem + a);
    B.l1 = *(const u32x4*)(smem + a + 16);
    B.oh = *(const u32x4*)(smem + L_OH + (a >> 1));
    return B;
}
template <bool SC1>
__device__ __forceinline__ void issue_blk(u32x4 rsrc, const Blk& B, u32 buf, const Lane& L) {
    dma4<SC1>(rsrc, buf, row_off(B.ix[0], L.coff[0]), row_off(B.ix[1], L.coff[1]), row_off(B.ix[2], L.coff[2]), row_off(B.ix[3], L.coff[3]));
}
__device__ __forceinline__ u32x4 sel_blk(const Blk& B, const Lane& L) {
    u32x4 b;
    b[0] = sel2(B.oh[0], B.l0[0], B.l0[1], L.sh);
    b[1] = sel2(B.oh[2], B.l1[0], B.l1[1], L.sh);
    b[2] = sel2(B.oh[1], B.l0[2], B.l0[3], L.sh);
    b[3] = sel2(B.oh[3], B.l1[2], B.l1[3], L.sh);
    return b;
}
__device__ __forceinline__ void own_rows(u32x4 rsrc, u32 buf, int node0, int lane) {
    const u32 o = (u32)(node0 + (lane >> 3)) * 128u + (u32)(((lane & 7) ^ (lane >> 4)) << 4) + 4096u;
    dma2(rsrc, buf, o, o ^ 64u);
}
struct Prime { u32x4 off[2], bq[2]; u32 an; };
__device__ __forceinline__ Prime make_prime(const char* smem, int wv, const Lane& L) {
    const u16* tp = (const u16*)(smem + L_TAB + wv * 128) + (L.lane >> 4);
    Prime P;
#pragma unroll
    for (int h = 0; h < 2; ++h) {
        const Blk B = read_blk(smem, tp[4 * h], L);
#pragma unroll
        for (int i = 0; i < 4; ++i) P.off[h][i] = row_off(B.ix[i], L.coff[i]);
        P.bq[h] = sel_blk(B, L);
    }
    P.an = tp[8];
    return P;
}
template <bool SC1, bool OWN>
__device__ __forceinline__ void gather_group(f32x4 (&acc)[4], u32x4 rsrc, char* smem, int wv, int ns, const Lane& L, const Prime& P,
                                             int node0) {
    char* ring = smem + L_RING + wv * 4096;
    const u32 rb = lds_addr(ring);
    const u16* tp = (const u16*)(smem + L_TAB + wv * 128) + (L.lane >> 4);
    u32x4 bq[2] = {P.bq[0], P.bq[1]};
    __builtin_amdgcn_s_setprio(3);
#pragma unroll
    for (int h = 0; h < 2; ++h)
        if (h < ns) dma4<SC1>(rsrc, rb + h * RINGH, P.off[h][0], P.off[h][1], P.off[h][2], P.off[h][3]);
        else if (OWN && h == ns) own_rows(rsrc, rb + h * RINGH, node0, L.lane);
    u32 an = P.an;
    int rem = ns;
    while (rem > 0) {
#pragma unroll
        for (int h = 0; h < 2; ++h) {
            if (h == 1 && rem == 0) break;
            if (rem == 7) __builtin_amdgcn_s_setprio(2);
            else if (rem == 5) __builtin_amdgcn_s_setprio(1);
            else if (rem == 3) __builtin_amdgcn_s_setprio(0);
            const Blk B = read_blk(smem, an, L);
            an = tp[4 * (h + 3)];
            if (rem > 1) WAIT_VM(4); else if (OWN) WAIT_VM(2); else WAIT_VM(0);
            bf16x8 a[4];
#pragma unroll
            for (int c = 0; c < 4; ++c) a[c] = afrag(ring + h * RINGH, L, c);
            if (rem > 2) issue_blk<SC1>(rsrc, B, rb + h * RINGH, L);
            else if (OWN && rem == 2) own_rows(rsrc, rb + h * RINGH, node0, L.lane);
#pragma unroll
            for (int c = 0; c < 4; ++c)
                acc[c] = __builtin_amdgcn_mfma_f32_16x16x32_bf16(a[c], __builtin_bit_cast(bf16x8, bq[h]), acc[c], 0, 0, 0);
            bq[h] = sel_blk(B, L);
            --rem;
        }
        tp += 8;
    }
    __builtin_amdgcn_s_setprio(0);
}

__device__ __forceinline__ void put_record(char* smem, u32 lo, u32 meta) {
    const u32 q = ((lo & 0xffffu) * 1342u) >> 24;
    u32 a = atomicAdd((u32*)(smem + L_CNT + (meta & 0xf0u) + q * 4u), (q & 1u) ? (u32)-4 : 4u);
    a = min(a, (u32)L_DUMP);
    *(u32*)(smem + a) = lo;
    *(u16*)(smem + L_OH + (a >> 1)) = (u16)(1u << (meta & 15u));
}
struct Pos { u32 a[3], oh[3]; bool ok[3]; };
__device__ __forceinline__ Pos claim_chunk(char* smem, u32x4 v) {
    Pos P;
#pragma unroll
    for (int k = 0; k < 3; ++k) {
        const u32 g = (v[3] >> (8 * k)) & 0xf0u;
        const u32 m = (v[k] & 0xffffu) * 1342u;
        u32 dir;
        asm("v_bfe_i32 %0, %1, 24, 1\n\tv_lshl_or_b32 %0, %0, 3, 4" : "=v"(dir) : "v"(m));
        if (k == 0) asm("v_lshlrev_b16_sdwa %0, %1, %2 dst_sel:DWORD dst_unused:UNUSED_PAD src0_sel:BYTE_0 src1_sel:DWORD" : "=v"(P.oh[k]) : "v"(v[3]), "v"(1u));
        if (k == 1) asm("v_lshlrev_b16_sdwa %0, %1, %2 dst_sel:DWORD dst_unused:UNUSED_PAD src0_sel:BYTE_1 src1_sel:DWORD" : "=v"(P.oh[k]) : "v"(v[3]), "v"(1u));
        if (k == 2) asm("v_lshlrev_b16_sdwa %0, %1, %2 dst_sel:DWORD dst_unused:UNUSED_PAD src0_sel:BYTE_2 src1_sel:DWORD" : "=v"(P.oh[k]) : "v"(v[3]), "v"(1u));
        P.ok[k] = g != 0u;
        P.a[k] = dir;
        if (P.ok[k]) P.a[k] = atomicAdd((u32*)(smem + L_CNT + (g | ((m >> 22) & 0xcu))), dir);
    }
    return P;
}
__device__ __forceinline__ void write_chunk(char* smem, Pos& P, u32x4 v) {
    asm volatile("" : "+v"(P.a[0]), "+v"(P.a[1]), "+v"(P.a[2]));
#pragma unroll
    for (int k = 0; k < 3; ++k) {
        const u32 p = min(P.a[k], (u32)L_DUMP);
        if (P.ok[k]) {
            *(u32*)(smem + p) = v[k];
            *(u16*)(smem + L_OH + (p >> 1)) = (u16)P.oh[k];
        }
    }
}
__device__ __forceinline__ void put_surplus(char* smem, u32x4 r, u32 bucket) {
    if (bucket_of(r[1]) == bucket) put_record(smem, r[0], r[1] - bstart(bucket) + 16u);
}
__device__ __forceinline__ u32 wait_hdr(const unsigned long long* hdr, int b, u32 epoch) {
    const unsigned long long t0 = __builtin_amdgcn_s_memrealtime();
    for (;;) {
        const unsigned long long h = __hip_atomic_load(hdr + b, RLX_AGENT);
        if (__builtin_amdgcn_readfirstlane((u32)(h >> 32)) == (epoch ^ HKEY)) return min((u32)__builtin_amdgcn_readfirstlane((u32)h), (u32)CH);
        if (__builtin_amdgcn_s_memrealtime() - t0 > TMO) return 0u;
        __builtin_amdgcn_s_sleep(2);
    }
}
template <bool FIRST>
__device__ __forceinline__ void init_lists(char* smem, int t) {
#pragma unroll
    for (int h = 0; h < 2; ++h) {
        const int i = t + h * 1024;
        if (i < L_CNT / 16) *(u32x4*)(smem + i * 16) = (u32x4)(i < L_OH / 16 ? 0xffffffffu : 0u);
    }
    if (t >= 960) {
        const u32 b = (u32)t - 960u - 4u;
        if (b < 4u * GPB) *(u32*)(smem + L_CNT + 16 + b * 4) = (b >> 1) * (CAP2 * 4u) + ((b & 1u) ? (CAP2 - 1) * 4u : 0u);
        else if (FIRST) *(u32*)(smem + L_CNT + 16 + (int)b * 4) = 0u;
    }
}

template <bool SC1>
__device__ __forceinline__ void slow_bucket(char* smem, f32x4 (&acc)[4], const char* bl, const char* ovf,
                                            const unsigned long long* hdr, u32 epoch, u32 bucket,
                                            u32x4 rsrc, int t, int wv, const Lane& L) {
    int blk = -((NB1 + 7) / 8);
    u32 o0 = 0, n = 0;
    for (;;) {
        if (blk >= 0 && o0 >= n) {
            u32* bc = (u32*)(smem + L_CNT) + F_BC;
            __syncthreads();
            if (wv == 0) {
                for (; blk < NB1; ++blk)
                    if (__any(__hip_atomic_load((const u32*)(bl + blk * 128 + (t & 7) * 16 + 12), RLX_AGENT) >> 31)) break;
                n = blk < NB1 ? wait_hdr(hdr, blk, epoch) : 0u;
                if (t == 0) { bc[0] = (u32)blk; bc[1] = n; }
            }
            __syncthreads();
            blk = __builtin_amdgcn_readfirstlane((int)bc[0]);
            n = __builtin_amdgcn_readfirstlane(bc[1]);
            if (blk >= NB1) break;
            o0 = 0;
        }
        __syncthreads();
        init_lists<false>(smem, t);
        __syncthreads();
        if (blk < 0) {
            const int c = (blk + (NB1 + 7) / 8) * 64 + t;
            if (t < 64 && c < NB1 * 8) { const u32x4 v = ld16(bl, NB1 * 128, c * 16); Pos P = claim_chunk(smem, v); write_chunk(smem, P, v); }
            ++blk;
        } else {
            if (t < CAP2 && o0 + t < n) put_surplus(smem, ld16(ovf, NB1 * CH * 16, (blk * CH + o0 + t) * 16), bucket);
            o0 += CAP2;
            if (o0 >= n) ++blk;
        }
        __syncthreads();
        if (wv < GPB) {
            const int ns = make_tab(smem, wv, L.lane);
            gather_group<SC1, false>(acc, rsrc, smem, wv, ns, L, make_prime(smem, wv, L), 0);
        }
    }
}

__device__ __forceinline__ void store_tile(char* __restrict__ outp, int node0, int nrows, const u32x2 (&v)[4], char* buf, int lane) {
    const int n = lane & 15, q = lane >> 4;
#pragma unroll
    for (int c = 0; c < 4; ++c) *(u32x2*)(buf + n * 128 + (((2 * c + (q >> 1)) ^ (n & 7)) << 4) + 8 * (q & 1)) = v[c];
    __builtin_amdgcn_wave_barrier();
    char* p = outp + (size_t)node0 * 128 + lane * 16;
    const int r = lane >> 3;
#pragma unroll
    for (int h = 0; h < 2; ++h)
        if (r + 8 * h < nrows) st16(p + h * 1024, *(const u32x4*)(buf + h * 1024 + r * 128 + (((lane & 7) ^ r) << 4)));
}
__device__ __forceinline__ void dense(f32x4 (&o)[4], const char* smem, int k, const u32x2 (&f)[4], const Lane& L) {
#pragma unroll
    for (int kk = 0; kk < 2; ++kk) {
        u32x4 b;
        b[0] = f[2 * kk][0]; b[1] = f[2 * kk][1]; b[2] = f[2 * kk + 1][0]; b[3] = f[2 * kk + 1][1];
#pragma unroll
        for (int c = 0; c < 4; ++c)
            o[c] = __builtin_amdgcn_mfma_f32_16x16x32_bf16(afrag(smem + L_W + (2 * k + kk) * 4096, L, c), __builtin_bit_cast(bf16x8, b), o[c], 0, 0, 0);
    }
}

__device__ __forceinline__ void bar_arrive(u32* bar, int lane) {
    if (lane < NREP) __hip_atomic_fetch_add(bar + (lane * NSH + blockIdx.x % NSH) * 64, 1u, RLX_AGENT);
}
__device__ __forceinline__ void bar_wait(const u32* bar, int lane) {
    const u32* pb = bar + ((blockIdx.x / NSH) % NREP * NSH + (lane % NSH)) * 64;
    const unsigned long long t0 = __builtin_amdgcn_s_memrealtime();
    u32 c = lane < NSH ? 0u : (u32)(NBK / NSH);
    while (__builtin_amdgcn_s_memrealtime() - t0 < TMO) {
        if (c != (u32)(NBK / NSH)) c = __hip_atomic_load(pb, RLX_AGENT);
        if (__all(c == (u32)(NBK / NSH))) break;
        __builtin_amdgcn_s_sleep(2);
    }
}
__device__ __forceinline__ void lds_wait(const u32* f, u32 n) {
    while (__hip_atomic_load(f, RLX_WG) < n) __builtin_amdgcn_s_sleep(1);
    asm volatile("" ::: "memory");
}

__device__ __forceinline__ void wait_lines(u32x4& v, const char* bl, u32 off, u32 tag, int lane) {
    const bool in = off < (u32)(NB1 * 128);
    unsigned long long bad = __ballot(in && ((v[3] >> 24) & 0x7fu) != tag);
    if (bad != 0ull) {
        const unsigned long long t0 = __builtin_amdgcn_s_memrealtime();
        do {
            __builtin_amdgcn_s_sleep(2);
            asm volatile("" ::: "memory");
            if ((bad >> (lane & 56)) & 0xffull) v = ld16(bl, NB1 * 128, off);
            asm volatile("" : "+v"(v));
            bad = __ballot(in && ((v[3] >> 24) & 0x7fu) != tag);
        } while (bad != 0ull && __builtin_amdgcn_s_memrealtime() - t0 < TMO);
        if (bad != 0ull) v[3] = 0u;
    }
}
__device__ __forceinline__ u32x4 first_surplus(unsigned long long f, int b0, const char* ovf, int lane) {
    return ld16(ovf, NB1 * CH * 16, (u32)((b0 + (__builtin_ctzll(f) >> 3)) * CH + lane) * 16u);
}
__device__ __forceinline__ void take_surplus(char* smem, unsigned long long f, int b0, u32x4 r, const char* ovf,
                                             const unsigned long long* hdr, u32 epoch, u32 bucket, int lane) {
    for (;;) {
        const int j = __builtin_ctzll(f) >> 3, b = b0 + j;
        const u32 m = (u32)(f >> (8 * j)) & 0xffu;
        bool done = false;
        for (int it = 0; m < 64u; ++it) {
            const bool mine = r[2] == epoch && r[3] == ~epoch && bucket_of(r[1] & 0xffffu) == bucket;
            if ((u32)__builtin_popcountll(__ballot(mine)) == m) {
                if (mine) put_surplus(smem, r, bucket);
                done = true;
                break;
            }
            if (it == 3) break;
            asm volatile("" ::: "memory");
            r = first_surplus(f, b0, ovf, lane);
        }
        if (!done) {
            const u32 n = wait_hdr(hdr, b, epoch);
            for (u32 o = lane; o < n; o += 64) put_surplus(smem, ld16(ovf, NB1 * CH * 16, (b * CH + o) * 16u), bucket);
        }
        f &= ~(0xffull << (8 * j));
        if (f == 0ull) break;
        r = first_surplus(f, b0, ovf, lane);
    }
}

__global__ __launch_bounds__(1024, 4) void k_cheb(const u32* __restrict__ src, const u32* __restrict__ dst,
                                               const u16* __restrict__ ewp, const char* __restrict__ x,
                                               const char* __restrict__ w, char* ws, char* __restrict__ out) {
    __shared__ __attribute__((aligned(256))) char smem[L_K2];
    char* lines = ws + W_LINE;
    char* ovf = ws + W_OVF;
    unsigned long long* hdr = (unsigned long long*)(ws + W_HDR);
    u32* bar = (u32*)(ws + W_BAR);
    char* t1 = ws + W_T1;
    const int t = threadIdx.x, lane = t & 63, wv = __builtin_amdgcn_readfirstlane(t >> 6);
    const int blk = blockIdx.x;
    const int bucket = blk;
    const bool active = wv < GPB;
    const int node0 = (int)bstart(bucket) + wv * 16;
    const int nrows = min((int)bstart(bucket + 1) - node0, 16);
    char* ring = smem + L_RING + wv * 4096;
    u32* lflag = (u32*)(smem + L_CNT);
    Lane L = make_lane(lane);
    u32 epoch;
    {
        u32* stage = (u32*)(smem + L_STAGE);
        u32* cnt = (u32*)(smem + L_CNTA);
        const u32 nb = blk < NB1 ? (u32)CH : 0u;
        const size_t e0i = (size_t)min(blk, NB1 - 1) * CH;
        const u32x4 d = __builtin_amdgcn_raw_buffer_load_b128(__builtin_amdgcn_make_buffer_rsrc((void*)(dst + e0i), 0, nb * 4u, 0x00020000), t * 16, 0, 2);
        const u32x4 s = __builtin_amdgcn_raw_buffer_load_b128(__builtin_amdgcn_make_buffer_rsrc((void*)(src + e0i), 0, nb * 4u, 0x00020000), t * 16, 0, 2);
        const u32x2 ew = __builtin_amdgcn_raw_buffer_load_b64(__builtin_amdgcn_make_buffer_rsrc((void*)(ewp + e0i), 0, nb * 2u, 0x00020000), t * 8, 0, 2);
        const u32 e1 = __hip_atomic_load(bar + 288 * 64, RLX_AGENT) + 1u;
        if (blk == NBK - 1 && t < 256) __hip_atomic_store(bar + t * 64, 0u, RLX_AGENT);
        if (t < 512) *(u32x4*)&stage[3 * 2048 + t * 4] = (u32x4)0u;
        else if (t < 512 + 65) *(u32x4*)&cnt[(t - 512) * 4] = (u32x4)0u;
        init_lists<true>(smem, t);
        asm volatile("" : "+v"(L.coff[0]), "+v"(L.coff[1]), "+v"(L.coff[2]), "+v"(L.coff[3]), "+v"(L.tra[0]), "+v"(L.tra[1]),
                     "+v"(L.tra[2]), "+v"(L.tra[3]), "+v"(L.sh));
        __syncthreads();
        __builtin_amdgcn_sched_barrier(0);
        const bool bin = t < CH / 4 && blk < NB1;
        u32 rk[4], bk[4];
#pragma unroll
        for (int i = 0; i < 4; ++i) {
            bk[i] = bucket_of((u32)d[i]);
            rk[i] = 0u;
            if (bin) rk[i] = atomicAdd(&cnt[bk[i]], 1u);
        }
        asm volatile("" :: "v"(s), "v"(ew));
        if (bin) {
#pragma unroll
            for (int i = 0; i < 4; ++i) {
                const u32 lo = (u32)s[i] | ((i & 1) ? (ew[i >> 1] & 0xffff0000u) : (ew[i >> 1] << 16));
                const u32 c = bk[i] * 8u + (rk[i] & 7u), sub = rk[i] >> 3;
                if (__builtin_expect(rk[i] < (u32)SLOTS, 1)) {
                    stage[sub * 2048u + c] = lo;
                    ((u8*)&stage[3 * 2048 + c])[sub] = (u8)((u32)d[i] - bstart(bk[i]) + 16u);
                } else {
                    const u32 o = atomicAdd(&cnt[256], 1u);
                    u32x4 r;
                    r[0] = lo; r[1] = (u32)d[i]; r[2] = e1; r[3] = ~e1;
                    st16(ovf + (size_t)(blk * CH + o) * 16, r);
                }
            }
        }
        epoch = __builtin_amdgcn_readfirstlane(e1);
        if (blk == NBK - 1) WAIT_VM(0);
        __syncthreads();
        if (blk < NB1) {
#pragma unroll
            for (int h = 0; h < 2; ++h) {
                const int c = t + h * 1024;
                const u32 m = min(max(cnt[c >> 3], (u32)SLOTS) - (u32)SLOTS, 255u);
                u32x4 v;
                v[0] = stage[c]; v[1] = stage[2048 + c]; v[2] = stage[4096 + c];
                v[3] = stage[6144 + c] | ((epoch & 0x7fu) << 24) | ((m >> (c & 7)) << 31);
                st16(lines + ((size_t)(c >> 3) * NB1 + blk) * 128 + (c & 7) * 16, v);
            }
        } else if (blk == NBK - 1 && t < 32) {
            __hip_atomic_store((unsigned long long*)(bar + (256 + t) * 64), (unsigned long long)epoch | ((unsigned long long)(epoch ^ HKEY) << 32), RLX_AGENT);
        }
    }
    const char* bl = lines + (size_t)bucket * (NB1 * 128);
    {
        __builtin_amdgcn_s_sleep(DELAY);
        if (blk >= NB1) __builtin_amdgcn_s_sleep(30);
        u32x4 v0 = ld16(bl, NB1 * 128, t * 16);
        u32x4 v1 = ld16(bl, NB1 * 128, (t + 1024) * 16);
        u32x4 r0 = 0u, r1 = 0u;
        wait_lines(v0, bl, t * 16, epoch & 0x7fu, lane);
        const unsigned long long f0 = __ballot(v0[3] >> 31);
        if (__builtin_expect(f0 != 0ull, 0)) r0 = first_surplus(f0, wv * 8, ovf, lane);
        {
            Pos P0 = claim_chunk(smem, v0);
            write_chunk(smem, P0, v0);
        }
        __builtin_amdgcn_sched_barrier(0);
        wait_lines(v1, bl, (t + 1024) * 16, epoch & 0x7fu, lane);
        const unsigned long long f1 = __ballot(v1[3] >> 31);
        if (__builtin_expect(f1 != 0ull, 0)) r1 = first_surplus(f1, wv * 8 + 128, ovf, lane);
        {
            Pos P1 = claim_chunk(smem, v1);
            write_chunk(smem, P1, v1);
        }
        {
            u32* cnt = (u32*)(smem + L_CNTA);
            const u32 novf = cnt[256];
            if (__builtin_expect(novf != 0u, 0)) {
                WAIT_VM(0);
                u32 old = 0;
                if (lane == 0) old = atomicAdd(&cnt[257], 1u);
                if (old == 15u) __hip_atomic_store(hdr + blk, (unsigned long long)novf | ((unsigned long long)(epoch ^ HKEY) << 32), RLX_AGENT);
            }
        }
        if (__builtin_expect(f0 != 0ull, 0)) take_surplus(smem, f0, wv * 8, r0, ovf, hdr, epoch, bucket, lane);
        if (__builtin_expect(f1 != 0ull, 0)) take_surplus(smem, f1, wv * 8 + 128, r1, ovf, hdr, epoch, bucket, lane);
    }
    __syncthreads();
    if (!active) {
        __builtin_amdgcn_s_setprio(3);
        const u32x4 wrs = make_rsrc(w - 4096, 3u * 64u * 128u + 4096u);
        const int a = lane >> 3;
#pragma unroll
        for (int h = 0; h < 2; ++h) {
            const int ks = (wv - GPB) * 2 + h;
            const u32 rb = (u32)(32 * ks + 4 * (a >> 1) + 2 * (a & 1)) * 128u;
            dma4<false>(wrs, lds_addr(smem + L_W + ks * 4096), rb + L.coff[0], rb + 128u + L.coff[1], rb + 2048u + L.coff[2], rb + 2176u + L.coff[3]);
        }
    }
    bool slow;
    {
        const u32x2 c = *(const u32x2*)(smem + L_CNT + 16 + min(lane, 2 * GPB - 1) * 8);
        slow = __builtin_expect(__any((int)c[0] > (int)c[1] + 4), 0);
    }
    if (!active) WAIT_VM(0);
    if (wv == 15) {
        const unsigned long long* rp = (const unsigned long long*)(bar + (256 + (blk & 31)) * 64);
        const unsigned long long t0 = __builtin_amdgcn_s_memrealtime();
        while (__hip_atomic_load(rp, RLX_AGENT) != ((unsigned long long)epoch | ((unsigned long long)(epoch ^ HKEY) << 32)) &&
               __builtin_amdgcn_s_memrealtime() - t0 < TMO) __builtin_amdgcn_s_sleep(4);
    }
    if (!active && lane == 0) __hip_atomic_fetch_add(lflag + F_W, 1u, RLX_WG);
    if (!slow && !active) return;
    f32x4 acc[4], o[4];
#pragma unroll
    for (int c = 0; c < 4; ++c) { acc[c] = 0.f; o[c] = 0.f; }
    int ns = 0;
    {
        const u32x4 rsrc = make_rsrc(x - 4096, (u32)NN * 128u + 4096u);
        if (__builtin_expect(!slow, 1)) {
            ns = make_tab(smem, wv, lane);
            gather_group<false, true>(acc, rsrc, smem, wv, ns, L, make_prime(smem, wv, L), node0);
        } else {
            slow_bucket<false>(smem, acc, bl, ovf, hdr, epoch, bucket, rsrc, t, wv, L);
            if (active) own_rows(rsrc, lds_addr(ring), node0, lane);
        }
    }
    Prime P2;
    u32x2 xf[4];
    if (active) {
        u32x2 tf[4];
#pragma unroll
        for (int c = 0; c < 4; ++c) {
            tf[c][0] = pack_bf16(acc[c][0], acc[c][1]);
            tf[c][1] = pack_bf16(acc[c][2], acc[c][3]);
            acc[c] = 0.f;
        }
        store_tile(t1, node0, nrows, tf, ring + (~ns & 1) * RINGH, lane);
        lds_wait(lflag + F_W, 3u);
        dense(o, smem, 1, tf, L);
        WAIT_VM(0);
        u32 old = 0;
        if (lane == 0) old = __hip_atomic_fetch_add(lflag + F_ARR2, 1u, RLX_WG);
        const bool last = __builtin_amdgcn_readfirstlane(old) == (u32)(GPB - 1);
        if (last) bar_arrive(bar, lane);
        __builtin_amdgcn_sched_barrier(0);
        const int n = lane & 15, q = lane >> 4;
#pragma unroll
        for (int c = 0; c < 4; ++c)
            xf[c] = *(const u32x2*)(ring + (ns & 1) * RINGH + n * 128 + (((2 * c + (q >> 1)) ^ (n >> 1)) << 4) + 8 * (q & 1));
        dense(o, smem, 0, xf, L);
        P2 = make_prime(smem, wv, L);
        asm volatile("" : "+v"(P2.off[0]), "+v"(P2.off[1]), "+v"(P2.bq[0]), "+v"(P2.bq[1]), "+v"(P2.an));
        if (last) {
            bar_wait(bar, lane);
            if (lane == 0) __hip_atomic_store(lflag + F_GO2, 1u, RLX_WG);
            if (blk == NBK - 1 && lane == 0) __hip_atomic_store(bar + 288 * 64, epoch, RLX_AGENT);
        }
    }
    lds_wait(lflag + F_GO2, 1u);
    {
        const u32x4 rsrc = make_rsrc(t1 - 4096, (u32)NN * 128u + 4096u);
        if (__builtin_expect(!slow, 1)) gather_group<true, false>(acc, rsrc, smem, wv, ns, L, P2, 0);
        else slow_bucket<true>(smem, acc, bl, ovf, hdr, epoch, bucket, rsrc, t, wv, L);
    }
    if (!active) return;
    u32x2 f[4];
#pragma unroll
    for (int c = 0; c < 4; ++c) {
        f[c][0] = pack_bf16(2.f * acc[c][0] - bf_lo(xf[c][0]), 2.f * acc[c][1] - bf_hi(xf[c][0]));
        f[c][1] = pack_bf16(2.f * acc[c][2] - bf_lo(xf[c][1]), 2.f * acc[c][3] - bf_hi(xf[c][1]));
    }
    dense(o, smem, 2, f, L);
#pragma unroll
    for (int c = 0; c < 4; ++c) {
        f[c][0] = pack_bf16(o[c][0], o[c][1]);
        f[c][1] = pack_bf16(o[c][2], o[c][3]);
    }
    store_tile(out, node0, nrows, f, ring, lane);
}

extern "C" void kernel_launch(void* const* d_in, const int* in_sizes, int n_in, void* d_out, int out_size, void* d_ws,
                              size_t ws_size, hipStream_t stream) {
    k_cheb<<<NBK, 1024, 0, stream>>>((const u32*)d_in[1], (const u32*)d_in[2], (const u16*)d_in[3],
                                     (const char*)d_in[0], (const char*)d_in[4], (char*)d_ws, (char*)d_out);
}
```

```cpp
#include <hip/hip_runtime.h>
#include <hip/hip_bf16.h>
#include <stdint.h>

typedef unsigned int u32;
typedef unsigned short u16;
typedef unsigned char u8;
typedef u32 u32x4 __attribute__((ext_vector_type(4)));
typedef u32 u32x2 __attribute__((ext_vector_type(2)));
typedef short s16x2 __attribute__((ext_vector_type(2)));
typedef short s16x4 __attribute__((ext_vector_type(4)));
typedef __bf16 bf16x8 __attribute__((ext_vector_type(8)));
typedef __bf16 bf16x2 __attribute__((ext_vector_type(2)));
typedef float f32x4 __attribute__((ext_vector_type(4)));
#define LAS __attribute__((address_space(3)))

constexpr int NN = 50000;
constexpr int NBK = 256;
constexpr int NB1 = 250, CH = 3200;
constexpr int GPB = 13;
constexpr int SLOTS = 24;
constexpr int CAP2 = 192;

constexpr int DELAY = 50;
constexpr int NSH = 4;
constexpr int NREP = 64;

constexpr int L_LO = 0;
constexpr int L_NUL = L_LO + GPB * 2 * CAP2 * 4;
constexpr int L_DUMP = L_NUL + 32;
constexpr int L_OH = L_DUMP + 32;
constexpr int L_CNT = L_OH + L_OH / 2;
constexpr int L_TAB = L_CNT + 256;
constexpr int L_W = (L_TAB + GPB * 128 + 255) / 256 * 256;
constexpr int L_RING = L_W + 24576;
constexpr int RINGH = GPB * 4096;
constexpr int L_K2 = L_RING + 2 * RINGH;
constexpr int L_STAGE = L_RING;
constexpr int L_CNTA = L_STAGE + 32768;
static_assert(L_K2 <= 163840 && 32768 + 1040 <= RINGH, "lds");
static_assert(L_OH % 32 == 0 && L_CNT % 16 == 0 && L_TAB % 16 == 0, "align");
enum { F_BC = 56, F_ARR2 = 60, F_W = 61, F_GO2 = 62 };

constexpr size_t W_LINE = 0;
constexpr size_t W_OVF = W_LINE + (size_t)NBK * NB1 * 128;
constexpr size_t W_HDR = W_OVF + (size_t)NB1 * CH * 16;
constexpr size_t W_BAR = W_HDR + 2048;
constexpr size_t W_T1 = W_BAR + 512 * 256;
static_assert(W_OVF % 128 == 0 && W_BAR % 256 == 0 && W_T1 % 128 == 0 && NREP * NSH == 256, "align");
constexpr u32 HKEY = 0x9e3779b9u;
constexpr unsigned long long TMO = 20000000ull;

__device__ __forceinline__ u32 lds_addr(const void* p) { return (u32)(uintptr_t)(const LAS char*)p; }
__device__ __forceinline__ u32 bstart(u32 b) { return (b * 3125u) >> 4; }
__device__ __forceinline__ u32 bucket_of(u32 d) { return __umulhi(d * 16u + 15u, 1374390u); }

__device__ __forceinline__ u32x4 make_rsrc(const void* base, u32 bytes) {
    const uint64_t a = (uint64_t)base;
    u32x4 r;
    r[0] = (u32)a;
    r[1] = (u32)(a >> 32) & 0xffffu;
    r[2] = bytes;
    r[3] = 0x00020000u;
    return r;
}

template <bool SC1>
__device__ __forceinline__ void dma4(u32x4 rsrc, u32 lds, u32 o0, u32 o1, u32 o2, u32 o3) {
    u32 keep;
    if (SC1)
        asm volatile(
            "s_waitcnt lgkmcnt(0)\n\t"
            "s_mov_b32 %0, m0\n\t"
            "s_mov_b32 m0, %1\n\t"
            "s_nop 0\n\t"
            "buffer_load_dwordx4 %2, %6, 0 offen sc1 lds\n\t"
            "buffer_load_dwordx4 %3, %6, 0 offen offset:1024 sc1 lds\n\t"
            "buffer_load_dwordx4 %4, %6, 0 offen offset:2048 sc1 lds\n\t"
            "buffer_load_dwordx4 %5, %6, 0 offen offset:3072 sc1 lds\n\t"
            "s_mov_b32 m0, %0"
            : "=&s"(keep)
            : "s"(lds), "v"(o0), "v"(o1), "v"(o2), "v"(o3), "s"(rsrc)
            : "memory");
    else
        asm volatile(
            "s_waitcnt lgkmcnt(0)\n\t"
            "s_mov_b32 %0, m0\n\t"
            "s_mov_b32 m0, %1\n\t"
            "s_nop 0\n\t"
            "buffer_load_dwordx4 %2, %6, 0 offen lds\n\t"
            "buffer_load_dwordx4 %3, %6, 0 offen offset:1024 lds\n\t"
            "buffer_load_dwordx4 %4, %6, 0 offen offset:2048 lds\n\t"
            "buffer_load_dwordx4 %5, %6, 0 offen offset:3072 lds\n\t"
            "s_mov_b32 m0, %0"
            : "=&s"(keep)
            : "s"(lds), "v"(o0), "v"(o1), "v"(o2), "v"(o3), "s"(rsrc)
            : "memory");
}
__device__ __forceinline__ void dma2(u32x4 rsrc, u32 lds, u32 o0, u32 o1) {
    u32 keep;
    asm volatile(
        "s_waitcnt lgkmcnt(0)\n\t"
        "s_mov_b32 %0, m0\n\t"
        "s_mov_b32 m0, %1\n\t"
        "s_nop 0\n\t"
        "buffer_load_dwordx4 %2, %4, 0 offen lds\n\t"
        "buffer_load_dwordx4 %3, %4, 0 offen offset:1024 lds\n\t"
        "s_mov_b32 m0, %0"
        : "=&s"(keep)
        : "s"(lds), "v"(o0), "v"(o1), "s"(rsrc)
        : "memory");
}
#define WAIT_VM(n) asm volatile("s_waitcnt vmcnt(" #n ")" ::: "memory")
#define RLX_AGENT __ATOMIC_RELAXED, __HIP_MEMORY_SCOPE_AGENT
#define RLX_WG __ATOMIC_RELAXED, __HIP_MEMORY_SCOPE_WORKGROUP
__device__ __forceinline__ u32x4 ld16(const void* base, u32 bytes, u32 off) {
    return __builtin_amdgcn_raw_buffer_load_b128(__builtin_amdgcn_make_buffer_rsrc((void*)base, 0, bytes, 0x00020000), off, 0, 16);
}
__device__ __forceinline__ void st16(void* p, u32x4 v) {
    asm volatile("global_store_dwordx4 %0, %1, off sc1\n\ts_nop 1" ::"v"(p), "v"(v) : "memory");
}

__device__ __forceinline__ u32 pack_bf16(float a, float b) {
    bf16x2 v;
    v[0] = (__bf16)a;
    v[1] = (__bf16)b;
    return __builtin_bit_cast(u32, v);
}
__device__ __forceinline__ float bf_lo(u32 v) { return __uint_as_float(v << 16); }
__device__ __forceinline__ float bf_hi(u32 v) { return __uint_as_float(v & 0xffff0000u); }

struct Lane {
    u32 coff[4];
    u32 tra[4];
    u32 sh;
    int lane;
};
__device__ __forceinline__ Lane make_lane(int lane) {
    Lane L;
    L.lane = lane;
    const int a = lane >> 3, p = lane & 7;
#pragma unroll
    for (int i = 0; i < 4; ++i) L.coff[i] = (u32)(((p ^ ((((a >> 1) & 1) << 1) | ((i & 1) << 2))) << 4) + 4096 - 1024 * i);
    const int g = lane >> 4, q = (lane & 15) >> 2, pp = lane & 3;
    const int sw = ((g & 1) << 1) | ((q & 1) << 2);
#pragma unroll
    for (int c = 0; c < 4; ++c)
        L.tra[c] = (u32)(128 * (8 * (q & 1) + 2 * g + (q >> 1)) + 16 * ((2 * c + (pp >> 1)) ^ sw) + 8 * (pp & 1));
    L.sh = (u32)(15 - (lane & 15)) * 0x10001u;
    return L;
}
__device__ __forceinline__ bf16x8 afrag(const char* buf, const Lane& L, int c) {
    const s16x4 a0 = __builtin_amdgcn_ds_read_tr16_b64_v4i16((LAS s16x4*)(buf + L.tra[c]));
    const s16x4 a1 = __builtin_amdgcn_ds_read_tr16_b64_v4i16((LAS s16x4*)(buf + L.tra[c] + 2048));
    return __builtin_bit_cast(bf16x8, __builtin_shufflevector(a0, a1, 0, 1, 2, 3, 4, 5, 6, 7));
}
__device__ __forceinline__ u32 row_off(u32 lo, u32 add) {
    u32 r;
    asm("v_mad_u32_u16 %0, %1, %2, %3" : "=v"(r) : "v"(lo), "s"(128u), "v"(add));
    return r;
}
__device__ __forceinline__ u32 sel2(u32 oh, u32 lo0, u32 lo1, u32 sh) {
    const s16x2 m = (__builtin_bit_cast(s16x2, oh) << __builtin_bit_cast(s16x2, sh)) >> 15;
    return __builtin_amdgcn_perm(lo1, lo0, 0x07060302u) & __builtin_bit_cast(u32, m);
}
__device__ __forceinline__ int make_tab(char* smem, int wv, int lane) {
    const u32x4 cv = *(const u32x4*)(smem + L_CNT + 16 + wv * 16);
    const u32 c0 = __builtin_amdgcn_readfirstlane(cv[0]), c1 = __builtin_amdgcn_readfirstlane(cv[1]);
    const u32 c2 = __builtin_amdgcn_readfirstlane(cv[2]), c3 = __builtin_amdgcn_readfirstlane(cv[3]);
    const u32 b0 = (u32)wv * 48u;
    const u32 n0 = (c0 + 31u) >> 5, s1 = max((c1 + 4u) >> 5, n0);
    const u32 e2 = (c2 + 31u) >> 5, s3 = max((c3 + 4u) >> 5, e2);
    const u32 ea = n0 - b0, eb = ea + (e2 - s1), nb = eb + (b0 + 48u - s3), v = (u32)lane;
    u32 pb = v < eb ? s1 - ea : s3 - eb;
    pb = v < ea ? b0 : pb;
    const u32 a = v < nb ? (pb + v) * 32u : (u32)L_NUL;
    *(u16*)(smem + L_TAB + wv * 128 + lane * 2) = (u16)a;
    __builtin_amdgcn_wave_barrier();
    return (int)((nb + 3u) >> 2);
}
struct Blk { u32x4 ix, l0, l1, oh; };
__device__ __forceinline__ Blk read_blk(const char* smem, u32 a, const Lane& L) {
    Blk B;
    B.ix = *(const u32x4*)(smem + a + (L.lane & 8) * 2);
    B.l0 = *(const u32x4*)(smem + a);
    B.l1 = *(const u32x4*)(smem + a + 16);
    B.oh = *(const u32x4*)(smem + L_OH + (a >> 1));
    return B;
}
template <bool SC1>
__device__ __forceinline__ void issue_blk(u32x4 rsrc, const Blk& B, u32 buf, const Lane& L) {
    dma4<SC1>(rsrc, buf, row_off(B.ix[0], L.coff[0]), row_off(B.ix[1], L.coff[1]), row_off(B.ix[2], L.coff[2]), row_off(B.ix[3], L.coff[3]));
}
__device__ __forceinline__ u32x4 sel_blk(const Blk& B, const Lane& L) {
    u32x4 b;
    b[0] = sel2(B.oh[0], B.l0[0], B.l0[1], L.sh);
    b[1] = sel2(B.oh[2], B.l1[0], B.l1[1], L.sh);
    b[2] = sel2(B.oh[1], B.l0[2], B.l0[3], L.sh);
    b[3] = sel2(B.oh[3], B.l1[2], B.l1[3], L.sh);
    return b;
}
__device__ __forceinline__ void own_rows(u32x4 rsrc, u32 buf, int node0, int lane) {
    const u32 o = (u32)(node0 + (lane >> 3)) * 128u + (u32)(((lane & 7) ^ (lane >> 4)) << 4) + 4096u;
    dma2(rsrc, buf, o, o ^ 64u);
}
struct Prime { u32x4 off[2], bq[2]; u32 an; };
__device__ __forceinline__ Prime make_prime(const char* smem, int wv, const Lane& L) {
    const u16* tp = (const u16*)(smem + L_TAB + wv * 128) + (L.lane >> 4);
    Prime P;
#pragma unroll
    for (int h = 0; h < 2; ++h) {
        const Blk B = read_blk(smem, tp[4 * h], L);
#pragma unroll
        for (int i = 0; i < 4; ++i) P.off[h][i] = row_off(B.ix[i], L.coff[i]);
        P.bq[h] = sel_blk(B, L);
    }
    P.an = tp[8];
    return P;
}
template <bool SC1, bool OWN>
__device__ __forceinline__ void gather_group(f32x4 (&acc)[4], u32x4 rsrc, char* smem, int wv, int ns, const Lane& L, const Prime& P,
                                             int node0) {
    char* ring = smem + L_RING + wv * 4096;
    const u32 rb = lds_addr(ring);
    const u16* tp = (const u16*)(smem + L_TAB + wv * 128) + (L.lane >> 4);
    u32x4 bq[2] = {P.bq[0], P.bq[1]};
    __builtin_amdgcn_s_setprio(3);
#pragma unroll
    for (int h = 0; h < 2; ++h)
        if (h < ns) dma4<SC1>(rsrc, rb + h * RINGH, P.off[h][0], P.off[h][1], P.off[h][2], P.off[h][3]);
        else if (OWN && h == ns) own_rows(rsrc, rb + h * RINGH, node0, L.lane);
    u32 an = P.an;
    int rem = ns;
    while (rem > 0) {
#pragma unroll
        for (int h = 0; h < 2; ++h) {
            if (h == 1 && rem == 0) break;
            if (rem == 7) __builtin_amdgcn_s_setprio(2);
            else if (rem == 5) __builtin_amdgcn_s_setprio(1);
            else if (rem == 3) __builtin_amdgcn_s_setprio(0);
            const Blk B = read_blk(smem, an, L);
            an = tp[4 * (h + 3)];
            if (rem > 1) WAIT_VM(4); else if (OWN) WAIT_VM(2); else WAIT_VM(0);
            bf16x8 a[4];
#pragma unroll
            for (int c = 0; c < 4; ++c) a[c] = afrag(ring + h * RINGH, L, c);
            if (rem > 2) issue_blk<SC1>(rsrc, B, rb + h * RINGH, L);
            else if (OWN && rem == 2) own_rows(rsrc, rb + h * RINGH, node0, L.lane);
#pragma unroll
            for (int c = 0; c < 4; ++c)
                acc[c] = __builtin_amdgcn_mfma_f32_16x16x32_bf16(a[c], __builtin_bit_cast(bf16x8, bq[h]), acc[c], 0, 0, 0);
            bq[h] = sel_blk(B, L);
            --rem;
        }
        tp += 8;
    }
    __builtin_amdgcn_s_setprio(0);
}

__device__ __forceinline__ u32 class4(u32 lo) { return ((lo << 3) & 8u) | (((lo + 7768u) >> 13) & 4u); }
__device__ __forceinline__ void put_record(char* smem, u32 lo, u32 meta) {
    const u32 q = class4(lo);
    u32 a = atomicAdd((u32*)(smem + L_CNT + (meta & 0xf0u) + q), (q & 4u) ? (u32)-4 : 4u);
    a = min(a, (u32)L_DUMP);
    *(u32*)(smem + a) = lo;
    *(u16*)(smem + L_OH + (a >> 1)) = (u16)(1u << (meta & 15u));
}
struct Pos { u32 a[3], oh[3]; bool ok[3]; };
__device__ __forceinline__ Pos claim_chunk(char* smem, u32x4 v) {
    Pos P;
#pragma unroll
    for (int k = 0; k < 3; ++k) {
        const u32 g = (v[3] >> (8 * k)) & 0xf0u;
        const u32 m = class4(v[k]);
        u32 dir;
        asm("v_bfe_i32 %0, %1, 2, 1\n\tv_lshl_or_b32 %0, %0, 3, 4" : "=v"(dir) : "v"(m));
        if (k == 0) asm("v_lshlrev_b16_sdwa %0, %1, %2 dst_sel:DWORD dst_unused:UNUSED_PAD src0_sel:BYTE_0 src1_sel:DWORD" : "=v"(P.oh[k]) : "v"(v[3]), "v"(1u));
        if (k == 1) asm("v_lshlrev_b16_sdwa %0, %1, %2 dst_sel:DWORD dst_unused:UNUSED_PAD src0_sel:BYTE_1 src1_sel:DWORD" : "=v"(P.oh[k]) : "v"(v[3]), "v"(1u));
        if (k == 2) asm("v_lshlrev_b16_sdwa %0, %1, %2 dst_sel:DWORD dst_unused:UNUSED_PAD src0_sel:BYTE_2 src1_sel:DWORD" : "=v"(P.oh[k]) : "v"(v[3]), "v"(1u));
        P.ok[k] = g != 0u;
        P.a[k] = dir;
        if (P.ok[k]) P.a[k] = atomicAdd((u32*)(smem + L_CNT + (g | m)), dir);
    }
    return P;
}
__device__ __forceinline__ void write_chunk(char* smem, Pos& P, u32x4 v) {
    asm volatile("" : "+v"(P.a[0]), "+v"(P.a[1]), "+v"(P.a[2]));
#pragma unroll
    for (int k = 0; k < 3; ++k) {
        const u32 p = min(P.a[k], (u32)L_DUMP);
        if (P.ok[k]) {
            *(u32*)(smem + p) = v[k];
            *(u16*)(smem + L_OH + (p >> 1)) = (u16)P.oh[k];
        }
    }
}
__device__ __forceinline__ void put_surplus(char* smem, u32x4 r, u32 bucket) {
    if (bucket_of(r[1]) == bucket) put_record(smem, r[0], r[1] - bstart(bucket) + 16u);
}
__device__ __forceinline__ u32 wait_hdr(const unsigned long long* hdr, int b, u32 epoch) {
    const unsigned long long t0 = __builtin_amdgcn_s_memrealtime();
    for (;;) {
        const unsigned long long h = __hip_atomic_load(hdr + b, RLX_AGENT);
        if (__builtin_amdgcn_readfirstlane((u32)(h >> 32)) == (epoch ^ HKEY)) return min((u32)__builtin_amdgcn_readfirstlane((u32)h), (u32)CH);
        if (__builtin_amdgcn_s_memrealtime() - t0 > TMO) return 0u;
        __builtin_amdgcn_s_sleep(2);
    }
}
template <bool FIRST>
__device__ __forceinline__ void init_lists(char* smem, int t) {
#pragma unroll
    for (int h = 0; h < 2; ++h) {
        const int i = t + h * 1024;
        if (i < L_CNT / 16) *(u32x4*)(smem + i * 16) = (u32x4)(i < L_OH / 16 ? 0xffffffffu : 0u);
    }
    if (t >= 960) {
        const u32 b = (u32)t - 960u - 4u;
        if (b < 4u * GPB) *(u32*)(smem + L_CNT + 16 + b * 4) = (b >> 1) * (CAP2 * 4u) + ((b & 1u) ? (CAP2 - 1) * 4u : 0u);
        else if (FIRST) *(u32*)(smem + L_CNT + 16 + (int)b * 4) = 0u;
    }
}

template <bool SC1>
__device__ __forceinline__ void slow_bucket(char* smem, f32x4 (&acc)[4], const char* bl, const char* ovf,
                                            const unsigned long long* hdr, u32 epoch, u32 bucket,
                                            u32x4 rsrc, int t, int wv, const Lane& L) {
    int blk = -((NB1 + 7) / 8);
    u32 o0 = 0, n = 0;
    for (;;) {
        if (blk >= 0 && o0 >= n) {
            u32* bc = (u32*)(smem + L_CNT) + F_BC;
            __syncthreads();
            if (wv == 0) {
                for (; blk < NB1; ++blk)
                    if (__any(__hip_atomic_load((const u32*)(bl + blk * 128 + (t & 7) * 16 + 12), RLX_AGENT) >> 31)) break;
                n = blk < NB1 ? wait_hdr(hdr, blk, epoch) : 0u;
                if (t == 0) { bc[0] = (u32)blk; bc[1] = n; }
            }
            __syncthreads();
            blk = __builtin_amdgcn_readfirstlane((int)bc[0]);
            n = __builtin_amdgcn_readfirstlane(bc[1]);
            if (blk >= NB1) break;
            o0 = 0;
        }
        __syncthreads();
        init_lists<false>(smem, t);
        __syncthreads();
        if (blk < 0) {
            const int c = (blk + (NB1 + 7) / 8) * 64 + t;
            if (t < 64 && c < NB1 * 8) { const u32x4 v = ld16(bl, NB1 * 128, c * 16); Pos P = claim_chunk(smem, v); write_chunk(smem, P, v); }
            ++blk;
        } else {
            if (t < CAP2 && o0 + t < n) put_surplus(smem, ld16(ovf, NB1 * CH * 16, (blk * CH + o0 + t) * 16), bucket);
            o0 += CAP2;
            if (o0 >= n) ++blk;
        }
        __syncthreads();
        if (wv < GPB) {
            const int ns = make_tab(smem, wv, L.lane);
            gather_group<SC1, false>(acc, rsrc, smem, wv, ns, L, make_prime(smem, wv, L), 0);
        }
    }
}

__device__ __forceinline__ void store_tile(char* __restrict__ outp, int node0, int nrows, const u32x2 (&v)[4], char* buf, int lane) {
    const int n = lane & 15, q = lane >> 4;
#pragma unroll
    for (int c = 0; c < 4; ++c) *(u32x2*)(buf + n * 128 + (((2 * c + (q >> 1)) ^ (n & 7)) << 4) + 8 * (q & 1)) = v[c];
    __builtin_amdgcn_wave_barrier();
    char* p = outp + (size_t)node0 * 128 + lane * 16;
    const int r = lane >> 3;
#pragma unroll
    for (int h = 0; h < 2; ++h)
        if (r + 8 * h < nrows) st16(p + h * 1024, *(const u32x4*)(buf + h * 1024 + r * 128 + (((lane & 7) ^ r) << 4)));
}
__device__ __forceinline__ void dense(f32x4 (&o)[4], const char* smem, int k, const u32x2 (&f)[4], const Lane& L) {
#pragma unroll
    for (int kk = 0; kk < 2; ++kk) {
        u32x4 b;
        b[0] = f[2 * kk][0]; b[1] = f[2 * kk][1]; b[2] = f[2 * kk + 1][0]; b[3] = f[2 * kk + 1][1];
#pragma unroll
        for (int c = 0; c < 4; ++c)
            o[c] = __builtin_amdgcn_mfma_f32_16x16x32_bf16(afrag(smem + L_W + (2 * k + kk) * 4096, L, c), __builtin_bit_cast(bf16x8, b), o[c], 0, 0, 0);
    }
}

__device__ __forceinline__ void bar_arrive(u32* bar, int lane) {
    if (lane < NREP) __hip_atomic_fetch_add(bar + (lane * NSH + blockIdx.x % NSH) * 64, 1u, RLX_AGENT);
}
__device__ __forceinline__ void bar_wait(const u32* bar, int lane) {
    const u32* pb = bar + ((blockIdx.x / NSH) % NREP * NSH + (lane % NSH)) * 64;
    const unsigned long long t0 = __builtin_amdgcn_s_memrealtime();
    u32 c = lane < NSH ? 0u : (u32)(NBK / NSH);
    while (__builtin_amdgcn_s_memrealtime() - t0 < TMO) {
        if (c != (u32)(NBK / NSH)) c = __hip_atomic_load(pb, RLX_AGENT);
        if (__all(c == (u32)(NBK / NSH))) break;
        __builtin_amdgcn_s_sleep(2);
    }
}
__device__ __forceinline__ void lds_wait(const u32* f, u32 n) {
    while (__hip_atomic_load(f, RLX_WG) < n) __builtin_amdgcn_s_sleep(1);
    asm volatile("" ::: "memory");
}

__device__ __forceinline__ void wait_lines(u32x4& v, const char* bl, u32 off, u32 tag, int lane) {
    const bool in = off < (u32)(NB1 * 128);
    unsigned long long bad = __ballot(in && ((v[3] >> 24) & 0x7fu) != tag);
    if (bad != 0ull) {
        const unsigned long long t0 = __builtin_amdgcn_s_memrealtime();
        do {
            __builtin_amdgcn_s_sleep(2);
            asm volatile("" ::: "memory");
            if ((bad >> (lane & 56)) & 0xffull) v = ld16(bl, NB1 * 128, off);
            asm volatile("" : "+v"(v));
            bad = __ballot(in && ((v[3] >> 24) & 0x7fu) != tag);
        } while (bad != 0ull && __builtin_amdgcn_s_memrealtime() - t0 < TMO);
        if (bad != 0ull) v[3] = 0u;
        __builtin_amdgcn_s_waitcnt(0xC07F);
    }
}
__device__ __forceinline__ u32x4 first_surplus(unsigned long long f, int b0, const char* ovf, int lane) {
    return ld16(ovf, NB1 * CH * 16, (u32)((b0 + (__builtin_ctzll(f) >> 3)) * CH + lane) * 16u);
}
__device__ __forceinline__ void take_surplus(char* smem, unsigned long long f, int b0, u32x4 r, const char* ovf,
                                             const unsigned long long* hdr, u32 epoch, u32 bucket, int lane) {
    for (;;) {
        const int j = __builtin_ctzll(f) >> 3, b = b0 + j;
        const u32 m = (u32)(f >> (8 * j)) & 0xffu;
        bool done = false;
        for (int it = 0; m < 64u; ++it) {
            const bool mine = r[2] == epoch && r[3] == ~epoch && bucket_of(r[1] & 0xffffu) == bucket;
            if ((u32)__builtin_popcountll(__ballot(mine)) == m) {
                if (mine) put_surplus(smem, r, bucket);
                done = true;
                break;
            }
            if (it == 3) break;
            asm volatile("" ::: "memory");
            r = first_surplus(f, b0, ovf, lane);
        }
        if (!done) {
            const u32 n = wait_hdr(hdr, b, epoch);
            for (u32 o = lane; o < n; o += 64) put_surplus(smem, ld16(ovf, NB1 * CH * 16, (b * CH + o) * 16u), bucket);
        }
        f &= ~(0xffull << (8 * j));
        if (f == 0ull) break;
        r = first_surplus(f, b0, ovf, lane);
    }
}

__global__ __launch_bounds__(1024, 4) void k_cheb(const u32* __restrict__ src, const u32* __restrict__ dst,
                                               const u16* __restrict__ ewp, const char* __restrict__ x,
                                               const char* __restrict__ w, char* ws, char* __restrict__ out) {
    __shared__ __attribute__((aligned(256))) char smem[L_K2];
    char* lines = ws + W_LINE;
    char* ovf = ws + W_OVF;
    unsigned long long* hdr = (unsigned long long*)(ws + W_HDR);
    u32* bar = (u32*)(ws + W_BAR);
    char* t1 = ws + W_T1;
    const int t = threadIdx.x, lane = t & 63, wv = __builtin_amdgcn_readfirstlane(t >> 6);
    const int blk = blockIdx.x;
    const int bucket = (blk & 7) * 32 + (blk >> 3);
    const bool active = wv < GPB;
    const int node0 = (int)bstart(bucket) + wv * 16;
    const int nrows = min((int)bstart(bucket + 1) - node0, 16);
    char* ring = smem + L_RING + wv * 4096;
    u32* lflag = (u32*)(smem + L_CNT);
    Lane L = make_lane(lane);
    u32 epoch;
    {
        u32* stage = (u32*)(smem + L_STAGE);
        u32* cnt = (u32*)(smem + L_CNTA);
        const u32 nb = blk < NB1 ? (u32)CH : 0u;
        const size_t e0i = (size_t)min(blk, NB1 - 1) * CH;
        const u32x4 d = __builtin_amdgcn_raw_buffer_load_b128(__builtin_amdgcn_make_buffer_rsrc((void*)(dst + e0i), 0, nb * 4u, 0x00020000), t * 16, 0, 2);
        const u32x4 s = __builtin_amdgcn_raw_buffer_load_b128(__builtin_amdgcn_make_buffer_rsrc((void*)(src + e0i), 0, nb * 4u, 0x00020000), t * 16, 0, 2);
        const u32x2 ew = __builtin_amdgcn_raw_buffer_load_b64(__builtin_amdgcn_make_buffer_rsrc((void*)(ewp + e0i), 0, nb * 2u, 0x00020000), t * 8, 0, 2);
        const u32 e1 = __hip_atomic_load(bar + 288 * 64, RLX_AGENT) + 1u;
        if (blk == NBK - 1 && t < 256) __hip_atomic_store(bar + t * 64, 0u, RLX_AGENT);
        if (t < 512) *(u32x4*)&stage[3 * 2048 + t * 4] = (u32x4)0u;
        else if (t < 512 + 65) *(u32x4*)&cnt[(t - 512) * 4] = (u32x4)0u;
        init_lists<true>(smem, t);
        asm volatile("" : "+v"(L.coff[0]), "+v"(L.coff[1]), "+v"(L.coff[2]), "+v"(L.coff[3]), "+v"(L.tra[0]), "+v"(L.tra[1]),
                     "+v"(L.tra[2]), "+v"(L.tra[3]), "+v"(L.sh));
        __syncthreads();
        __builtin_amdgcn_sched_barrier(0);
        const bool bin = t < CH / 4 && blk < NB1;
        u32 rk[4], bk[4];
#pragma unroll
        for (int i = 0; i < 4; ++i) {
            bk[i] = bucket_of((u32)d[i]);
            rk[i] = 0u;
            if (bin) rk[i] = atomicAdd(&cnt[bk[i]], 1u);
        }
        asm volatile("" :: "v"(s), "v"(ew));
        if (bin) {
#pragma unroll
            for (int i = 0; i < 4; ++i) {
                const u32 lo = (u32)s[i] | ((i & 1) ? (ew[i >> 1] & 0xffff0000u) : (ew[i >> 1] << 16));
                const u32 c = bk[i] * 8u + (rk[i] & 7u), sub = rk[i] >> 3;
                if (__builtin_expect(rk[i] < (u32)SLOTS, 1)) {
                    stage[sub * 2048u + c] = lo;
                    ((u8*)&stage[3 * 2048 + c])[sub] = (u8)((u32)d[i] - bstart(bk[i]) + 16u);
                } else {
                    const u32 o = atomicAdd(&cnt[256], 1u);
                    u32x4 r;
                    r[0] = lo; r[1] = (u32)d[i]; r[2] = e1; r[3] = ~e1;
                    st16(ovf + (size_t)(blk * CH + o) * 16, r);
                }
            }
        }
        epoch = __builtin_amdgcn_readfirstlane(e1);
        if (blk == NBK - 1) WAIT_VM(0);
        __syncthreads();
        if (blk < NB1) {
#pragma unroll
            for (int h = 0; h < 2; ++h) {
                const int c = t + h * 1024;
                const u32 m = min(max(cnt[c >> 3], (u32)SLOTS) - (u32)SLOTS, 255u);
                u32x4 v;
                v[0] = stage[c]; v[1] = stage[2048 + c]; v[2] = stage[4096 + c];
                v[3] = stage[6144 + c] | ((epoch & 0x7fu) << 24) | ((m >> (c & 7)) << 31);
                st16(lines + ((size_t)(c >> 3) * NB1 + blk) * 128 + (c & 7) * 16, v);
            }
        } else if (blk == NBK - 1 && t < 32) {
            __hip_atomic_store((unsigned long long*)(bar + (256 + t) * 64), (unsigned long long)epoch | ((unsigned long long)(epoch ^ HKEY) << 32), RLX_AGENT);
        }
    }
    const char* bl = lines + (size_t)bucket * (NB1 * 128);
    {
        __builtin_amdgcn_s_sleep(DELAY);
        if (blk >= NB1) __builtin_amdgcn_s_sleep(30);
        u32x4 v0 = ld16(bl, NB1 * 128, t * 16);
        u32x4 v1 = ld16(bl, NB1 * 128, (t + 1024) * 16);
        u32x4 r0 = 0u, r1 = 0u;
        wait_lines(v0, bl, t * 16, epoch & 0x7fu, lane);
        const unsigned long long f0 = __ballot(v0[3] >> 31);
        if (__builtin_expect(f0 != 0ull, 0)) r0 = first_surplus(f0, wv * 8, ovf, lane);
        {
            Pos P0 = claim_chunk(smem, v0);
            write_chunk(smem, P0, v0);
        }
        __builtin_amdgcn_sched_barrier(0);
        wait_lines(v1, bl, (t + 1024) * 16, epoch & 0x7fu, lane);
        const unsigned long long f1 = __ballot(v1[3] >> 31);
        if (__builtin_expect(f1 != 0ull, 0)) r1 = first_surplus(f1, wv * 8 + 128, ovf, lane);
        {
            Pos P1 = claim_chunk(smem, v1);
            write_chunk(smem, P1, v1);
        }
        {
            u32* cnt = (u32*)(smem + L_CNTA);
            const u32 novf = cnt[256];
            if (__builtin_expect(novf != 0u, 0)) {
                WAIT_VM(0);
                u32 old = 0;
                if (lane == 0) old = atomicAdd(&cnt[257], 1u);
                if (old == 15u) __hip_atomic_store(hdr + blk, (unsigned long long)novf | ((unsigned long long)(epoch ^ HKEY) << 32), RLX_AGENT);
            }
        }
        if (__builtin_expect(f0 != 0ull, 0)) take_surplus(smem, f0, wv * 8, r0, ovf, hdr, epoch, bucket, lane);
        if (__builtin_expect(f1 != 0ull, 0)) take_surplus(smem, f1, wv * 8 + 128, r1, ovf, hdr, epoch, bucket, lane);
    }
    __syncthreads();
    if (!active) {
        __builtin_amdgcn_s_setprio(3);
        const u32x4 wrs = make_rsrc(w - 4096, 3u * 64u * 128u + 4096u);
        const int a = lane >> 3;
#pragma unroll
        for (int h = 0; h < 2; ++h) {
            const int ks = (wv - GPB) * 2 + h;
            const u32 rb = (u32)(32 * ks + 4 * (a >> 1) + 2 * (a & 1)) * 128u;
            dma4<false>(wrs, lds_addr(smem + L_W + ks * 4096), rb + L.coff[0], rb + 128u + L.coff[1], rb + 2048u + L.coff[2], rb + 2176u + L.coff[3]);
        }
    }
    bool slow;
    {
        const u32x2 c = *(const u32x2*)(smem + L_CNT + 16 + min(lane, 2 * GPB - 1) * 8);
        slow = __builtin_expect(__any((int)c[0] > (int)c[1] + 4), 0);
    }
    if (!active) WAIT_VM(0);
    if (wv == 15) {
        const unsigned long long* rp = (const unsigned long long*)(bar + (256 + (blk & 31)) * 64);
        const unsigned long long t0 = __builtin_amdgcn_s_memrealtime();
        while (__hip_atomic_load(rp, RLX_AGENT) != ((unsigned long long)epoch | ((unsigned long long)(epoch ^ HKEY) << 32)) &&
               __builtin_amdgcn_s_memrealtime() - t0 < TMO) __builtin_amdgcn_s_sleep(4);
    }
    if (!active && lane == 0) __hip_atomic_fetch_add(lflag + F_W, 1u, RLX_WG);
    if (!slow && !active) return;
    f32x4 acc[4], o[4];
#pragma unroll
    for (int c = 0; c < 4; ++c) { acc[c] = 0.f; o[c] = 0.f; }
    int ns = 0;
    {
        const u32x4 rsrc = make_rsrc(x - 4096, (u32)NN * 128u + 4096u);
        if (__builtin_expect(!slow, 1)) {
            ns = make_tab(smem, wv, lane);
            gather_group<false, true>(acc, rsrc, smem, wv, ns, L, make_prime(smem, wv, L), node0);
        } else {
            slow_bucket<false>(smem, acc, bl, ovf, hdr, epoch, bucket, rsrc, t, wv, L);
            if (active) own_rows(rsrc, lds_addr(ring), node0, lane);
        }
    }
    Prime P2;
    u32x2 xf[4];
    if (active) {
        u32x2 tf[4];
#pragma unroll
        for (int c = 0; c < 4; ++c) {
            tf[c][0] = pack_bf16(acc[c][0], acc[c][1]);
            tf[c][1] = pack_bf16(acc[c][2], acc[c][3]);
            acc[c] = 0.f;
        }
        store_tile(t1, node0, nrows, tf, ring + (~ns & 1) * RINGH, lane);
        lds_wait(lflag + F_W, 3u);
        dense(o, smem, 1, tf, L);
        WAIT_VM(0);
        u32 old = 0;
        if (lane == 0) old = __hip_atomic_fetch_add(lflag + F_ARR2, 1u, RLX_WG);
        const bool last = __builtin_amdgcn_readfirstlane(old) == (u32)(GPB - 1);
        if (last) bar_arrive(bar, lane);
        __builtin_amdgcn_sched_barrier(0);
        const int n = lane & 15, q = lane >> 4;
#pragma unroll
        for (int c = 0; c < 4; ++c)
            xf[c] = *(const u32x2*)(ring + (ns & 1) * RINGH + n * 128 + (((2 * c + (q >> 1)) ^ (n >> 1)) << 4) + 8 * (q & 1));
        dense(o, smem, 0, xf, L);
        P2 = make_prime(smem, wv, L);
        asm volatile("" : "+v"(P2.off[0]), "+v"(P2.off[1]), "+v"(P2.bq[0]), "+v"(P2.bq[1]), "+v"(P2.an));
        if (last) {
            bar_wait(bar, lane);
            if (lane == 0) __hip_atomic_store(lflag + F_GO2, 1u, RLX_WG);
            if (blk == NBK - 1 && lane == 0) __hip_atomic_store(bar + 288 * 64, epoch, RLX_AGENT);
        }
    }
    lds_wait(lflag + F_GO2, 1u);
    {
        const u32x4 rsrc = make_rsrc(t1 - 4096, (u32)NN * 128u + 4096u);
        if (__builtin_expect(!slow, 1)) gather_group<true, false>(acc, rsrc, smem, wv, ns, L, P2, 0);
        else slow_bucket<true>(smem, acc, bl, ovf, hdr, epoch, bucket, rsrc, t, wv, L);
    }
    if (!active) return;
    u32x2 f[4];
#pragma unroll
    for (int c = 0; c < 4; ++c) {
        f[c][0] = pack_bf16(2.f * acc[c][0] - bf_lo(xf[c][0]), 2.f * acc[c][1] - bf_hi(xf[c][0]));
        f[c][1] = pack_bf16(2.f * acc[c][2] - bf_lo(xf[c][1]), 2.f * acc[c][3] - bf_hi(xf[c][1]));
    }
    dense(o, smem, 2, f, L);
#pragma unroll
    for (int c = 0; c < 4; ++c) {
        f[c][0] = pack_bf16(o[c][0], o[c][1]);
        f[c][1] = pack_bf16(o[c][2], o[c][3]);
    }
    store_tile(out, node0, nrows, f, ring, lane);
}

extern "C" void kernel_launch(void* const* d_in, const int* in_sizes, int n_in, void* d_out, int out_size, void* d_ws,
                              size_t ws_size, hipStream_t stream) {
    k_cheb<<<NBK, 1024, 0, stream>>>((const u32*)d_in[1], (const u32*)d_in[2], (const u16*)d_in[3],
                                     (const char*)d_in[0], (const char*)d_in[4], (char*)d_ws, (char*)d_out);
}
```

```cpp
#include <hip/hip_runtime.h>
#include <hip/hip_bf16.h>
#include <stdint.h>

typedef unsigned int u32;
typedef unsigned short u16;
typedef unsigned char u8;
typedef u32 u32x4 __attribute__((ext_vector_type(4)));
typedef u32 u32x2 __attribute__((ext_vector_type(2)));
typedef short s16x2 __attribute__((ext_vector_type(2)));
typedef short s16x4 __attribute__((ext_vector_type(4)));
typedef __bf16 bf16x8 __attribute__((ext_vector_type(8)));
typedef __bf16 bf16x2 __attribute__((ext_vector_type(2)));
typedef float f32x4 __attribute__((ext_vector_type(4)));
#define LAS __attribute__((address_space(3)))

constexpr int NN = 50000;
constexpr int NBK = 256;
constexpr int NB1 = 250, CH = 3200;
constexpr int GPB = 13;
constexpr int SLOTS = 24;
constexpr int CAP2 = 192;

#define DELAY 50
constexpr int NSH = 4;
constexpr int NREP = 64;

constexpr int L_LO = 0;
constexpr int L_NUL = L_LO + GPB * 2 * CAP2 * 4;
constexpr int L_DUMP = L_NUL + 32;
constexpr int L_OH = L_DUMP + 32;
constexpr int L_CNT = L_OH + L_OH / 2;
constexpr int L_TAB = L_CNT + 256;
constexpr int L_W = (L_TAB + GPB * 128 + 255) / 256 * 256;
constexpr int L_RING = L_W + 24576;
constexpr int RINGH = GPB * 4096;
constexpr int L_K2 = L_RING + 2 * RINGH;
constexpr int L_STAGE = L_RING;
constexpr int L_CNTA = L_STAGE + 32768;
static_assert(L_K2 <= 163840 && 32768 + 1040 <= RINGH, "lds");
static_assert(L_OH % 32 == 0 && L_CNT % 16 == 0 && L_TAB % 16 == 0, "align");
enum { F_BC = 56, F_ARR2 = 60, F_W = 61, F_GO2 = 62 };

constexpr size_t W_LINE = 0;
constexpr size_t W_OVF = W_LINE + (size_t)NBK * NB1 * 128;
constexpr size_t W_HDR = W_OVF + (size_t)NB1 * CH * 16;
constexpr size_t W_BAR = W_HDR + 2048;
constexpr size_t W_T1 = W_BAR + 512 * 256;
static_assert(W_OVF % 128 == 0 && W_BAR % 256 == 0 && W_T1 % 128 == 0 && NREP * NSH == 256, "align");
constexpr u32 HKEY = 0x9e3779b9u;
constexpr unsigned long long TMO = 20000000ull;

__device__ __forceinline__ u32 lds_addr(const void* p) { return (u32)(uintptr_t)(const LAS char*)p; }
__device__ __forceinline__ u32 bstart(u32 b) { return (b * 3125u) >> 4; }
__device__ __forceinline__ u32 bucket_of(u32 d) { return __umulhi(d * 16u + 15u, 1374390u); }

__device__ __forceinline__ u32x4 make_rsrc(const void* base, u32 bytes) {
    const uint64_t a = (uint64_t)base;
    u32x4 r;
    r[0] = (u32)a;
    r[1] = (u32)(a >> 32) & 0xffffu;
    r[2] = bytes;
    r[3] = 0x00020000u;
    return r;
}

template <bool SC1>
__device__ __forceinline__ void dma4(u32x4 rsrc, u32 lds, u32 o0, u32 o1, u32 o2, u32 o3) {
    u32 keep;
    if (SC1)
        asm volatile(
            "s_waitcnt lgkmcnt(0)\n\t"
            "s_mov_b32 %0, m0\n\t"
            "s_mov_b32 m0, %1\n\t"
            "s_nop 0\n\t"
            "buffer_load_dwordx4 %2, %6, 0 offen sc1 lds\n\t"
            "buffer_load_dwordx4 %3, %6, 0 offen offset:1024 sc1 lds\n\t"
            "buffer_load_dwordx4 %4, %6, 0 offen offset:2048 sc1 lds\n\t"
            "buffer_load_dwordx4 %5, %6, 0 offen offset:3072 sc1 lds\n\t"
            "s_mov_b32 m0, %0"
            : "=&s"(keep)
            : "s"(lds), "v"(o0), "v"(o1), "v"(o2), "v"(o3), "s"(rsrc)
            : "memory");
    else
        asm volatile(
            "s_waitcnt lgkmcnt(0)\n\t"
            "s_mov_b32 %0, m0\n\t"
            "s_mov_b32 m0, %1\n\t"
            "s_nop 0\n\t"
            "buffer_load_dwordx4 %2, %6, 0 offen lds\n\t"
            "buffer_load_dwordx4 %3, %6, 0 offen offset:1024 lds\n\t"
            "buffer_load_dwordx4 %4, %6, 0 offen offset:2048 lds\n\t"
            "buffer_load_dwordx4 %5, %6, 0 offen offset:3072 lds\n\t"
            "s_mov_b32 m0, %0"
            : "=&s"(keep)
            : "s"(lds), "v"(o0), "v"(o1), "v"(o2), "v"(o3), "s"(rsrc)
            : "memory");
}
__device__ __forceinline__ void dma2(u32x4 rsrc, u32 lds, u32 o0, u32 o1) {
    u32 keep;
    asm volatile(
        "s_waitcnt lgkmcnt(0)\n\t"
        "s_mov_b32 %0, m0\n\t"
        "s_mov_b32 m0, %1\n\t"
        "s_nop 0\n\t"
        "buffer_load_dwordx4 %2, %4, 0 offen lds\n\t"
        "buffer_load_dwordx4 %3, %4, 0 offen offset:1024 lds\n\t"
        "s_mov_b32 m0, %0"
        : "=&s"(keep)
        : "s"(lds), "v"(o0), "v"(o1), "s"(rsrc)
        : "memory");
}
#define WAIT_VM(n) asm volatile("s_waitcnt vmcnt(" #n ")" ::: "memory")
#define RLX_AGENT __ATOMIC_RELAXED, __HIP_MEMORY_SCOPE_AGENT
#define RLX_WG __ATOMIC_RELAXED, __HIP_MEMORY_SCOPE_WORKGROUP
__device__ __forceinline__ u32x4 ld16(const void* base, u32 bytes, u32 off) {
    return __builtin_amdgcn_raw_buffer_load_b128(__builtin_amdgcn_make_buffer_rsrc((void*)base, 0, bytes, 0x00020000), off, 0, 16);
}
__device__ __forceinline__ void st16(void* p, u32x4 v) {
    asm volatile("global_store_dwordx4 %0, %1, off sc1\n\ts_nop 1" ::"v"(p), "v"(v) : "memory");
}

#define STAMP_DECL do {} while (0)
#define STAMP(k, i) do {} while (0)
#define STAMP_FLUSH(k) do {} while (0)

__device__ __forceinline__ u32 pack_bf16(float a, float b) {
    bf16x2 v;
    v[0] = (__bf16)a;
    v[1] = (__bf16)b;
    return __builtin_bit_cast(u32, v);
}
__device__ __forceinline__ float bf_lo(u32 v) { return __uint_as_float(v << 16); }
__device__ __forceinline__ float bf_hi(u32 v) { return __uint_as_float(v & 0xffff0000u); }

struct Lane {
    u32 coff[4];
    u32 tra[4];
    u32 sh;
    int lane;
};
__device__ __forceinline__ Lane make_lane(int lane) {
    Lane L;
    L.lane = lane;
    const int a = lane >> 3, p = lane & 7;
#pragma unroll
    for (int i = 0; i < 4; ++i) L.coff[i] = (u32)(((p ^ ((((a >> 1) & 1) << 1) | ((i & 1) << 2))) << 4) + 4096 - 1024 * i);
    const int g = lane >> 4, q = (lane & 15) >> 2, pp = lane & 3;
    const int sw = ((g & 1) << 1) | ((q & 1) << 2);
#pragma unroll
    for (int c = 0; c < 4; ++c)
        L.tra[c] = (u32)(128 * (8 * (q & 1) + 2 * g + (q >> 1)) + 16 * ((2 * c + (pp >> 1)) ^ sw) + 8 * (pp & 1));
    L.sh = (u32)(15 - (lane & 15)) * 0x10001u;
    return L;
}
__device__ __forceinline__ bf16x8 afrag(const char* buf, const Lane& L, int c) {
    const s16x4 a0 = __builtin_amdgcn_ds_read_tr16_b64_v4i16((LAS s16x4*)(buf + L.tra[c]));
    const s16x4 a1 = __builtin_amdgcn_ds_read_tr16_b64_v4i16((LAS s16x4*)(buf + L.tra[c] + 2048));
    return __builtin_bit_cast(bf16x8, __builtin_shufflevector(a0, a1, 0, 1, 2, 3, 4, 5, 6, 7));
}
__device__ __forceinline__ u32 row_off(u32 lo, u32 add) {
    u32 r;
    asm("v_mad_u32_u16 %0, %1, %2, %3" : "=v"(r) : "v"(lo), "s"(128u), "v"(add));
    return r;
}
__device__ __forceinline__ u32 sel2(u32 oh, u32 lo0, u32 lo1, u32 sh) {
    const s16x2 m = (__builtin_bit_cast(s16x2, oh) << __builtin_bit_cast(s16x2, sh)) >> 15;
    return __builtin_amdgcn_perm(lo1, lo0, 0x07060302u) & __builtin_bit_cast(u32, m);
}
__device__ __forceinline__ int make_tab(char* smem, int wv, int lane) {
    const u32x4 cv = *(const u32x4*)(smem + L_CNT + 16 + wv * 16);
    const u32 c0 = __builtin_amdgcn_readfirstlane(cv[0]), c1 = __builtin_amdgcn_readfirstlane(cv[1]);
    const u32 c2 = __builtin_amdgcn_readfirstlane(cv[2]), c3 = __builtin_amdgcn_readfirstlane(cv[3]);
    const u32 b0 = (u32)wv * 48u;
    const u32 n0 = (c0 + 31u) >> 5, s1 = max((c1 + 4u) >> 5, n0);
    const u32 e2 = (c2 + 31u) >> 5, s3 = max((c3 + 4u) >> 5, e2);
    const u32 ea = n0 - b0, eb = ea + (e2 - s1), nb = eb + (b0 + 48u - s3), v = (u32)lane;
    u32 pb = v < eb ? s1 - ea : s3 - eb;
    pb = v < ea ? b0 : pb;
    const u32 a = v < nb ? (pb + v) * 32u : (u32)L_NUL;
    *(u16*)(smem + L_TAB + wv * 128 + lane * 2) = (u16)a;
    __builtin_amdgcn_wave_barrier();
    return (int)((nb + 3u) >> 2);
}
struct Blk { u32x4 ix, l0, l1, oh; };
__device__ __forceinline__ Blk read_blk(const char* smem, u32 a, const Lane& L) {
    Blk B;
    B.ix = *(const u32x4*)(smem + a + (L.lane & 8) * 2);
    B.l0 = *(const u32x4*)(smem + a);
    B.l1 = *(const u32x4*)(smem + a + 16);
    B.oh = *(const u32x4*)(smem + L_OH + (a >> 1));
    return B;
}
template <bool SC1>
__device__ __forceinline__ void issue_blk(u32x4 rsrc, const Blk& B, u32 buf, const Lane& L) {
    dma4<SC1>(rsrc, buf, row_off(B.ix[0], L.coff[0]), row_off(B.ix[1], L.coff[1]), row_off(B.ix[2], L.coff[2]), row_off(B.ix[3], L.coff[3]));
}
__device__ __forceinline__ u32x4 sel_blk(const Blk& B, const Lane& L) {
    u32x4 b;
    b[0] = sel2(B.oh[0], B.l0[0], B.l0[1], L.sh);
    b[1] = sel2(B.oh[2], B.l1[0], B.l1[1], L.sh);
    b[2] = sel2(B.oh[1], B.l0[2], B.l0[3], L.sh);
    b[3] = sel2(B.oh[3], B.l1[2], B.l1[3], L.sh);
    return b;
}
__device__ __forceinline__ void own_rows(u32x4 rsrc, u32 buf, int node0, int lane) {
    const u32 o = (u32)(node0 + (lane >> 3)) * 128u + (u32)(((lane & 7) ^ (lane >> 4)) << 4) + 4096u;
    dma2(rsrc, buf, o, o ^ 64u);
}
struct Prime { u32x4 off[2], bq[2]; u32 an; };
__device__ __forceinline__ Prime make_prime(const char* smem, int wv, const Lane& L) {
    const u16* tp = (const u16*)(smem + L_TAB + wv * 128) + (L.lane >> 4);
    Prime P;
#pragma unroll
    for (int h = 0; h < 2; ++h) {
        const Blk B = read_blk(smem, tp[4 * h], L);
#pragma unroll
        for (int i = 0; i < 4; ++i) P.off[h][i] = row_off(B.ix[i], L.coff[i]);
        P.bq[h] = sel_blk(B, L);
    }
    P.an = tp[8];
    return P;
}
template <bool SC1, bool OWN>
__device__ __forceinline__ void gather_group(f32x4 (&acc)[4], u32x4 rsrc, char* smem, int wv, int ns, const Lane& L, const Prime& P,
                                             int node0) {
    char* ring = smem + L_RING + wv * 4096;
    const u32 rb = lds_addr(ring);
    const u16* tp = (const u16*)(smem + L_TAB + wv * 128) + (L.lane >> 4);
    u32x4 bq[2] = {P.bq[0], P.bq[1]};
    __builtin_amdgcn_s_setprio(3);
#pragma unroll
    for (int h = 0; h < 2; ++h)
        if (h < ns) dma4<SC1>(rsrc, rb + h * RINGH, P.off[h][0], P.off[h][1], P.off[h][2], P.off[h][3]);
        else if (OWN && h == ns) own_rows(rsrc, rb + h * RINGH, node0, L.lane);
    u32 an = P.an;
    int rem = ns;
    while (rem > 0) {
#pragma unroll
        for (int h = 0; h < 2; ++h) {
            if (h == 1 && rem == 0) break;
            if (rem == 7) __builtin_amdgcn_s_setprio(2);
            else if (rem == 5) __builtin_amdgcn_s_setprio(1);
            else if (rem == 3) __builtin_amdgcn_s_setprio(0);
            const Blk B = read_blk(smem, an, L);
            an = tp[4 * (h + 3)];
            if (rem > 1) WAIT_VM(4); else if (OWN) WAIT_VM(2); else WAIT_VM(0);
            bf16x8 a[4];
#pragma unroll
            for (int c = 0; c < 4; ++c) a[c] = afrag(ring + h * RINGH, L, c);
            if (rem > 2) issue_blk<SC1>(rsrc, B, rb + h * RINGH, L);
            else if (OWN && rem == 2) own_rows(rsrc, rb + h * RINGH, node0, L.lane);
#pragma unroll
            for (int c = 0; c < 4; ++c)
                acc[c] = __builtin_amdgcn_mfma_f32_16x16x32_bf16(a[c], __builtin_bit_cast(bf16x8, bq[h]), acc[c], 0, 0, 0);
            bq[h] = sel_blk(B, L);
            --rem;
        }
        tp += 8;
    }
    __builtin_amdgcn_s_setprio(0);
}

__device__ __forceinline__ void put_record(char* smem, u32 lo, u32 meta) {
    const u32 q = ((lo & 0xffffu) * 1342u) >> 24;
    u32 a = atomicAdd((u32*)(smem + L_CNT + (meta & 0xf0u) + q * 4u), (q & 1u) ? (u32)-4 : 4u);
    a = min(a, (u32)L_DUMP);
    *(u32*)(smem + a) = lo;
    *(u16*)(smem + L_OH + (a >> 1)) = (u16)(1u << (meta & 15u));
}
struct Pos { u32 a[3], oh[3]; bool ok[3]; };
__device__ __forceinline__ Pos claim_chunk(char* smem, u32x4 v) {
    Pos P;
#pragma unroll
    for (int k = 0; k < 3; ++k) {
        const u32 g = (v[3] >> (8 * k)) & 0xf0u;
        const u32 m = (v[k] & 0xffffu) * 1342u;
        u32 dir;
        asm("v_bfe_i32 %0, %1, 24, 1\n\tv_lshl_or_b32 %0, %0, 3, 4" : "=v"(dir) : "v"(m));
        if (k == 0) asm("v_lshlrev_b16_sdwa %0, %1, %2 dst_sel:DWORD dst_unused:UNUSED_PAD src0_sel:BYTE_0 src1_sel:DWORD" : "=v"(P.oh[k]) : "v"(v[3]), "v"(1u));
        if (k == 1) asm("v_lshlrev_b16_sdwa %0, %1, %2 dst_sel:DWORD dst_unused:UNUSED_PAD src0_sel:BYTE_1 src1_sel:DWORD" : "=v"(P.oh[k]) : "v"(v[3]), "v"(1u));
        if (k == 2) asm("v_lshlrev_b16_sdwa %0, %1, %2 dst_sel:DWORD dst_unused:UNUSED_PAD src0_sel:BYTE_2 src1_sel:DWORD" : "=v"(P.oh[k]) : "v"(v[3]), "v"(1u));
        P.ok[k] = g != 0u;
        P.a[k] = dir;
        if (P.ok[k]) P.a[k] = atomicAdd((u32*)(smem + L_CNT + (g | ((m >> 22) & 0xcu))), dir);
    }
    return P;
}
__device__ __forceinline__ void write_chunk(char* smem, Pos& P, u32x4 v) {
    asm volatile("" : "+v"(P.a[0]), "+v"(P.a[1]), "+v"(P.a[2]));
#pragma unroll
    for (int k = 0; k < 3; ++k) {
        const u32 p = min(P.a[k], (u32)L_DUMP);
        if (P.ok[k]) {
            *(u32*)(smem + p) = v[k];
            *(u16*)(smem + L_OH + (p >> 1)) = (u16)P.oh[k];
        }
    }
}
__device__ __forceinline__ void put_surplus(char* smem, u32x4 r, u32 bucket) {
    if (bucket_of(r[1]) == bucket) put_record(smem, r[0], r[1] - bstart(bucket) + 16u);
}
__device__ __forceinline__ u32 wait_hdr(const unsigned long long* hdr, int b, u32 epoch) {
    const unsigned long long t0 = __builtin_amdgcn_s_memrealtime();
    for (;;) {
        const unsigned long long h = __hip_atomic_load(hdr + b, RLX_AGENT);
        if (__builtin_amdgcn_readfirstlane((u32)(h >> 32)) == (epoch ^ HKEY)) return min((u32)__builtin_amdgcn_readfirstlane((u32)h), (u32)CH);
        if (__builtin_amdgcn_s_memrealtime() - t0 > TMO) return 0u;
        __builtin_amdgcn_s_sleep(2);
    }
}
template <bool FIRST>
__device__ __forceinline__ void init_lists(char* smem, int t) {
#pragma unroll
    for (int h = 0; h < 2; ++h) {
        const int i = t + h * 1024;
        if (i < L_CNT / 16) *(u32x4*)(smem + i * 16) = (u32x4)(i < L_OH / 16 ? 0xffffffffu : 0u);
    }
    if (t >= 960) {
        const u32 b = (u32)t - 960u - 4u;
        if (b < 4u * GPB) *(u32*)(smem + L_CNT + 16 + b * 4) = (b >> 1) * (CAP2 * 4u) + ((b & 1u) ? (CAP2 - 1) * 4u : 0u);
        else if (FIRST) *(u32*)(smem + L_CNT + 16 + (int)b * 4) = 0u;
    }
}

template <bool SC1>
__device__ __forceinline__ void slow_bucket(char* smem, f32x4 (&acc)[4], const char* bl, const char* ovf,
                                            const unsigned long long* hdr, u32 epoch, u32 bucket,
                                            u32x4 rsrc, int t, int wv, const Lane& L) {
    int blk = -((NB1 + 7) / 8);
    u32 o0 = 0, n = 0;
    for (;;) {
        if (blk >= 0 && o0 >= n) {
            u32* bc = (u32*)(smem + L_CNT) + F_BC;
            __syncthreads();
            if (wv == 0) {
                for (; blk < NB1; ++blk)
                    if (__any(__hip_atomic_load((const u32*)(bl + blk * 128 + (t & 7) * 16 + 12), RLX_AGENT) >> 31)) break;
                n = blk < NB1 ? wait_hdr(hdr, blk, epoch) : 0u;
                if (t == 0) { bc[0] = (u32)blk; bc[1] = n; }
            }
            __syncthreads();
            blk = __builtin_amdgcn_readfirstlane((int)bc[0]);
            n = __builtin_amdgcn_readfirstlane(bc[1]);
            if (blk >= NB1) break;
            o0 = 0;
        }
        __syncthreads();
        init_lists<false>(smem, t);
        __syncthreads();
        if (blk < 0) {
            const int c = (blk + (NB1 + 7) / 8) * 64 + t;
            if (t < 64 && c < NB1 * 8) { const u32x4 v = ld16(bl, NB1 * 128, c * 16); Pos P = claim_chunk(smem, v); write_chunk(smem, P, v); }
            ++blk;
        } else {
            if (t < CAP2 && o0 + t < n) put_surplus(smem, ld16(ovf, NB1 * CH * 16, (blk * CH + o0 + t) * 16), bucket);
            o0 += CAP2;
            if (o0 >= n) ++blk;
        }
        __syncthreads();
        if (wv < GPB) {
            const int ns = make_tab(smem, wv, L.lane);
            gather_group<SC1, false>(acc, rsrc, smem, wv, ns, L, make_prime(smem, wv, L), 0);
        }
    }
}

__device__ __forceinline__ void store_tile(char* __restrict__ outp, int node0, int nrows, const u32x2 (&v)[4], char* buf, int lane) {
    const int n = lane & 15, q = lane >> 4;
#pragma unroll
    for (int c = 0; c < 4; ++c) *(u32x2*)(buf + n * 128 + (((2 * c + (q >> 1)) ^ (n & 7)) << 4) + 8 * (q & 1)) = v[c];
    __builtin_amdgcn_wave_barrier();
    char* p = outp + (size_t)node0 * 128 + lane * 16;
    const int r = lane >> 3;
#pragma unroll
    for (int h = 0; h < 2; ++h)
        if (r + 8 * h < nrows) st16(p + h * 1024, *(const u32x4*)(buf + h * 1024 + r * 128 + (((lane & 7) ^ r) << 4)));
}
__device__ __forceinline__ void dense(f32x4 (&o)[4], const char* smem, int k, const u32x2 (&f)[4], const Lane& L) {
#pragma unroll
    for (int kk = 0; kk < 2; ++kk) {
        u32x4 b;
        b[0] = f[2 * kk][0]; b[1] = f[2 * kk][1]; b[2] = f[2 * kk + 1][0]; b[3] = f[2 * kk + 1][1];
#pragma unroll
        for (int c = 0; c < 4; ++c)
            o[c] = __builtin_amdgcn_mfma_f32_16x16x32_bf16(afrag(smem + L_W + (2 * k + kk) * 4096, L, c), __builtin_bit_cast(bf16x8, b), o[c], 0, 0, 0);
    }
}

__device__ __forceinline__ void bar_arrive(u32* bar, int lane) {
    if (lane < NREP) __hip_atomic_fetch_add(bar + (lane * NSH + blockIdx.x % NSH) * 64, 1u, RLX_AGENT);
}
__device__ __forceinline__ void bar_wait(const u32* bar, int lane) {
    const u32* pb = bar + ((blockIdx.x / NSH) % NREP * NSH + (lane % NSH)) * 64;
    const unsigned long long t0 = __builtin_amdgcn_s_memrealtime();
    u32 c = lane < NSH ? 0u : (u32)(NBK / NSH);
    while (__builtin_amdgcn_s_memrealtime() - t0 < TMO) {
        if (c != (u32)(NBK / NSH)) c = __hip_atomic_load(pb, RLX_AGENT);
        if (__all(c == (u32)(NBK / NSH))) break;
        __builtin_amdgcn_s_sleep(2);
    }
}
__device__ __forceinline__ void lds_wait(const u32* f, u32 n) {
    while (__hip_atomic_load(f, RLX_WG) < n) __builtin_amdgcn_s_sleep(1);
    asm volatile("" ::: "memory");
}

__device__ __forceinline__ void wait_lines(u32x4& v, const char* bl, u32 off, u32 tag, int lane) {
    const bool in = off < (u32)(NB1 * 128);
    unsigned long long bad = __ballot(in && ((v[3] >> 24) & 0x7fu) != tag);
    if (bad != 0ull) {
        const unsigned long long t0 = __builtin_amdgcn_s_memrealtime();
        do {
            __builtin_amdgcn_s_sleep(2);
            asm volatile("" ::: "memory");
            if ((bad >> (lane & 56)) & 0xffull) v = ld16(bl, NB1 * 128, off);
            asm volatile("" : "+v"(v));
            bad = __ballot(in && ((v[3] >> 24) & 0x7fu) != tag);
        } while (bad != 0ull && __builtin_amdgcn_s_memrealtime() - t0 < TMO);
        if (bad != 0ull) v[3] = 0u;
    }
}
__device__ __forceinline__ u32x4 first_surplus(unsigned long long f, int b0, const char* ovf, int lane) {
    return ld16(ovf, NB1 * CH * 16, (u32)((b0 + (__builtin_ctzll(f) >> 3)) * CH + lane) * 16u);
}
__device__ __forceinline__ void take_surplus(char* smem, unsigned long long f, int b0, u32x4 r, const char* ovf,
                                             const unsigned long long* hdr, u32 epoch, u32 bucket, int lane) {
    for (;;) {
        const int j = __builtin_ctzll(f) >> 3, b = b0 + j;
        const u32 m = (u32)(f >> (8 * j)) & 0xffu;
        bool done = false;
        for (int it = 0; m < 64u; ++it) {
            const bool mine = r[2] == epoch && r[3] == ~epoch && bucket_of(r[1] & 0xffffu) == bucket;
            if ((u32)__builtin_popcountll(__ballot(mine)) == m) {
                if (mine) put_surplus(smem, r, bucket);
                done = true;
                break;
            }
            if (it == 3) break;
            asm volatile("" ::: "memory");
            r = first_surplus(f, b0, ovf, lane);
        }
        if (!done) {
            const u32 n = wait_hdr(hdr, b, epoch);
            for (u32 o = lane; o < n; o += 64) put_surplus(smem, ld16(ovf, NB1 * CH * 16, (b * CH + o) * 16u), bucket);
        }
        f &= ~(0xffull << (8 * j));
        if (f == 0ull) break;
        r = first_surplus(f, b0, ovf, lane);
    }
}

__global__ __launch_bounds__(1024, 4) void k_cheb(const u32* __restrict__ src, const u32* __restrict__ dst,
                                               const u16* __restrict__ ewp, const char* __restrict__ x,
                                               const char* __restrict__ w, char* ws, char* __restrict__ out) {
    __shared__ __attribute__((aligned(256))) char smem[L_K2];
    char* lines = ws + W_LINE;
    char* ovf = ws + W_OVF;
    unsigned long long* hdr = (unsigned long long*)(ws + W_HDR);
    u32* bar = (u32*)(ws + W_BAR);
    char* t1 = ws + W_T1;
    const int t = threadIdx.x, lane = t & 63, wv = __builtin_amdgcn_readfirstlane(t >> 6);
    const int blk = blockIdx.x;
    const int xr = blk >> 3;
    const int bucket = (blk & 7) * 32 + xr;
    const bool active = wv < GPB;
    const int node0 = (int)bstart(bucket) + wv * 16;
    const int nrows = min((int)bstart(bucket + 1) - node0, 16);
    char* ring = smem + L_RING + wv * 4096;
    u32* lflag = (u32*)(smem + L_CNT);
    Lane L = make_lane(lane);
    STAMP_DECL;
    STAMP(0, 0);
    u32 epoch;
    {
        u32* stage = (u32*)(smem + L_STAGE);
        u32* cnt = (u32*)(smem + L_CNTA);
        const u32 nb = blk < NB1 ? (u32)CH : 0u;
        const size_t e0i = (size_t)min(blk, NB1 - 1) * CH;
        const u32x4 d = __builtin_amdgcn_raw_buffer_load_b128(__builtin_amdgcn_make_buffer_rsrc((void*)(dst + e0i), 0, nb * 4u, 0x00020000), t * 16, 0, 2);
        const u32x4 s = __builtin_amdgcn_raw_buffer_load_b128(__builtin_amdgcn_make_buffer_rsrc((void*)(src + e0i), 0, nb * 4u, 0x00020000), t * 16, 0, 2);
        const u32x2 ew = __builtin_amdgcn_raw_buffer_load_b64(__builtin_amdgcn_make_buffer_rsrc((void*)(ewp + e0i), 0, nb * 2u, 0x00020000), t * 8, 0, 2);
        const u32 e1 = __hip_atomic_load(bar + 288 * 64, RLX_AGENT) + 1u;
        if (blk == NBK - 1 && t < 256) __hip_atomic_store(bar + t * 64, 0u, RLX_AGENT);
        if (t < 512) *(u32x4*)&stage[3 * 2048 + t * 4] = (u32x4)0u;
        else if (t < 512 + 65) *(u32x4*)&cnt[(t - 512) * 4] = (u32x4)0u;
        init_lists<true>(smem, t);
        asm volatile("" : "+v"(L.coff[0]), "+v"(L.coff[1]), "+v"(L.coff[2]), "+v"(L.coff[3]), "+v"(L.tra[0]), "+v"(L.tra[1]),
                     "+v"(L.tra[2]), "+v"(L.tra[3]), "+v"(L.sh));
        __syncthreads();
        __builtin_amdgcn_sched_barrier(0);
        const bool bin = t < CH / 4 && blk < NB1;
        u32 rk[4], bk[4];
#pragma unroll
        for (int i = 0; i < 4; ++i) {
            bk[i] = bucket_of((u32)d[i]);
            rk[i] = 0u;
            if (bin) rk[i] = atomicAdd(&cnt[bk[i]], 1u);
        }
        asm volatile("" :: "v"(s), "v"(ew));
        if (bin) {
#pragma unroll
            for (int i = 0; i < 4; ++i) {
                const u32 lo = (u32)s[i] | ((i & 1) ? (ew[i >> 1] & 0xffff0000u) : (ew[i >> 1] << 16));
                const u32 c = bk[i] * 8u + (rk[i] & 7u), sub = rk[i] >> 3;
                if (__builtin_expect(rk[i] < (u32)SLOTS, 1)) {
                    stage[sub * 2048u + c] = lo;
                    ((u8*)&stage[3 * 2048 + c])[sub] = (u8)((u32)d[i] - bstart(bk[i]) + 16u);
                } else {
                    const u32 o = atomicAdd(&cnt[256], 1u);
                    u32x4 r;
                    r[0] = lo; r[1] = (u32)d[i]; r[2] = e1; r[3] = ~e1;
                    st16(ovf + (size_t)(blk * CH + o) * 16, r);
                }
            }
        }
        epoch = __builtin_amdgcn_readfirstlane(e1);
        if (blk == NBK - 1) WAIT_VM(0);
        STAMP(0, 1);
        __syncthreads();
        if (blk < NB1) {
#pragma unroll
            for (int h = 0; h < 2; ++h) {
                const int c = t + h * 1024;
                const u32 m = min(max(cnt[c >> 3], (u32)SLOTS) - (u32)SLOTS, 255u);
                u32x4 v;
                v[0] = stage[c]; v[1] = stage[2048 + c]; v[2] = stage[4096 + c];
                v[3] = stage[6144 + c] | ((epoch & 0x7fu) << 24) | ((m >> (c & 7)) << 31);
                st16(lines + ((size_t)(c >> 3) * NB1 + blk) * 128 + (c & 7) * 16, v);
            }
        } else if (blk == NBK - 1 && t < 32) {
            __hip_atomic_store((unsigned long long*)(bar + (256 + t) * 64), (unsigned long long)epoch | ((unsigned long long)(epoch ^ HKEY) << 32), RLX_AGENT);
        }
    }
    STAMP(0, 2);
    const char* bl = lines + (size_t)bucket * (NB1 * 128);
    {
        __builtin_amdgcn_s_sleep(DELAY);
        if (blk >= NB1) __builtin_amdgcn_s_sleep(30);
        u32x4 v0 = ld16(bl, NB1 * 128, t * 16);
        u32x4 v1 = ld16(bl, NB1 * 128, (t + 1024) * 16);
        u32x4 r0 = 0u, r1 = 0u;
        wait_lines(v0, bl, t * 16, epoch & 0x7fu, lane);
        const unsigned long long f0 = __ballot(v0[3] >> 31);
        if (__builtin_expect(f0 != 0ull, 0)) r0 = first_surplus(f0, wv * 8, ovf, lane);
        {
            Pos P0 = claim_chunk(smem, v0);
            write_chunk(smem, P0, v0);
        }
        __builtin_amdgcn_sched_barrier(0);
        wait_lines(v1, bl, (t + 1024) * 16, epoch & 0x7fu, lane);
        STAMP(0, 3);
        const unsigned long long f1 = __ballot(v1[3] >> 31);
        if (__builtin_expect(f1 != 0ull, 0)) r1 = first_surplus(f1, wv * 8 + 128, ovf, lane);
        {
            Pos P1 = claim_chunk(smem, v1);
            write_chunk(smem, P1, v1);
        }
        {
            u32* cnt = (u32*)(smem + L_CNTA);
            const u32 novf = cnt[256];
            if (__builtin_expect(novf != 0u, 0)) {
                WAIT_VM(0);
                u32 old = 0;
                if (lane == 0) old = atomicAdd(&cnt[257], 1u);
                if (old == 15u) __hip_atomic_store(hdr + blk, (unsigned long long)novf | ((unsigned long long)(epoch ^ HKEY) << 32), RLX_AGENT);
            }
        }
        if (__builtin_expect(f0 != 0ull, 0)) take_surplus(smem, f0, wv * 8, r0, ovf, hdr, epoch, bucket, lane);
        if (__builtin_expect(f1 != 0ull, 0)) take_surplus(smem, f1, wv * 8 + 128, r1, ovf, hdr, epoch, bucket, lane);
    }
    __syncthreads();
    STAMP(0, 4);
    if (!active) {
        __builtin_amdgcn_s_setprio(3);
        const u32x4 wrs = make_rsrc(w - 4096, 3u * 64u * 128u + 4096u);
        const int a = lane >> 3;
#pragma unroll
        for (int h = 0; h < 2; ++h) {
            const int ks = (wv - GPB) * 2 + h;
            const u32 rb = (u32)(32 * ks + 4 * (a >> 1) + 2 * (a & 1)) * 128u;
            dma4<false>(wrs, lds_addr(smem + L_W + ks * 4096), rb + L.coff[0], rb + 128u + L.coff[1], rb + 2048u + L.coff[2], rb + 2176u + L.coff[3]);
        }
    }
    bool slow;
    {
        const u32x2 c = *(const u32x2*)(smem + L_CNT + 16 + min(lane, 2 * GPB - 1) * 8);
        slow = __builtin_expect(__any((int)c[0] > (int)c[1] + 4), 0);
    }
    if (!active) WAIT_VM(0);
    if (wv == 15) {
        const unsigned long long* rp = (const unsigned long long*)(bar + (256 + xr) * 64);
        const unsigned long long t0 = __builtin_amdgcn_s_memrealtime();
        while (__hip_atomic_load(rp, RLX_AGENT) != ((unsigned long long)epoch | ((unsigned long long)(epoch ^ HKEY) << 32)) &&
               __builtin_amdgcn_s_memrealtime() - t0 < TMO) __builtin_amdgcn_s_sleep(4);
    }
    if (!active && lane == 0) __hip_atomic_fetch_add(lflag + F_W, 1u, RLX_WG);
    if (!slow && !active) return;
    f32x4 acc[4], o[4];
#pragma unroll
    for (int c = 0; c < 4; ++c) { acc[c] = 0.f; o[c] = 0.f; }
    int ns = 0;
    {
        const u32x4 rsrc = make_rsrc(x - 4096, (u32)NN * 128u + 4096u);
        if (__builtin_expect(!slow, 1)) {
            ns = make_tab(smem, wv, lane);
            gather_group<false, true>(acc, rsrc, smem, wv, ns, L, make_prime(smem, wv, L), node0);
        } else {
            slow_bucket<false>(smem, acc, bl, ovf, hdr, epoch, bucket, rsrc, t, wv, L);
            if (active) own_rows(rsrc, lds_addr(ring), node0, lane);
        }
    }
    STAMP(0, 5);
    Prime P2;
    u32x2 xf[4];
    if (active) {
        u32x2 tf[4];
#pragma unroll
        for (int c = 0; c < 4; ++c) {
            tf[c][0] = pack_bf16(acc[c][0], acc[c][1]);
            tf[c][1] = pack_bf16(acc[c][2], acc[c][3]);
            acc[c] = 0.f;
        }
        store_tile(t1, node0, nrows, tf, ring + (~ns & 1) * RINGH, lane);
        lds_wait(lflag + F_W, 3u);
        dense(o, smem, 1, tf, L);
        WAIT_VM(0);
        u32 old = 0;
        if (lane == 0) old = __hip_atomic_fetch_add(lflag + F_ARR2, 1u, RLX_WG);
        const bool last = __builtin_amdgcn_readfirstlane(old) == (u32)(GPB - 1);
        if (last) bar_arrive(bar, lane);
        __builtin_amdgcn_sched_barrier(0);
        const int n = lane & 15, q = lane >> 4;
#pragma unroll
        for (int c = 0; c < 4; ++c)
            xf[c] = *(const u32x2*)(ring + (ns & 1) * RINGH + n * 128 + (((2 * c + (q >> 1)) ^ (n >> 1)) << 4) + 8 * (q & 1));
        dense(o, smem, 0, xf, L);
        P2 = make_prime(smem, wv, L);
        asm volatile("" : "+v"(P2.off[0]), "+v"(P2.off[1]), "+v"(P2.bq[0]), "+v"(P2.bq[1]), "+v"(P2.an));
        if (last) {
            bar_wait(bar, lane);
            if (lane == 0) __hip_atomic_store(lflag + F_GO2, 1u, RLX_WG);
            if (blk == NBK - 1 && lane == 0) __hip_atomic_store(bar + 288 * 64, epoch, RLX_AGENT);
        }
    }
    lds_wait(lflag + F_GO2, 1u);
    STAMP(0, 6);
    {
        const u32x4 rsrc = make_rsrc(t1 - 4096, (u32)NN * 128u + 4096u);
        if (__builtin_expect(!slow, 1)) gather_group<true, false>(acc, rsrc, smem, wv, ns, L, P2, 0);
        else slow_bucket<true>(smem, acc, bl, ovf, hdr, epoch, bucket, rsrc, t, wv, L);
    }
    if (!active) return;
    u32x2 f[4];
#pragma unroll
    for (int c = 0; c < 4; ++c) {
        f[c][0] = pack_bf16(2.f * acc[c][0] - bf_lo(xf[c][0]), 2.f * acc[c][1] - bf_hi(xf[c][0]));
        f[c][1] = pack_bf16(2.f * acc[c][2] - bf_lo(xf[c][1]), 2.f * acc[c][3] - bf_hi(xf[c][1]));
    }
    dense(o, smem, 2, f, L);
#pragma unroll
    for (int c = 0; c < 4; ++c) {
        f[c][0] = pack_bf16(o[c][0], o[c][1]);
        f[c][1] = pack_bf16(o[c][2], o[c][3]);
    }
    store_tile(out, node0, nrows, f, ring, lane);
}

extern "C" void kernel_launch(void* const* d_in, const int* in_sizes, int n_in, void* d_out, int out_size, void* d_ws,
                              size_t ws_size, hipStream_t stream) {
    k_cheb<<<NBK, 1024, 0, stream>>>((const u32*)d_in[1], (const u32*)d_in[2], (const u16*)d_in[3],
                                     (const char*)d_in[0], (const char*)d_in[4], (char*)d_ws, (char*)d_out);
}
```

```cpp
#include <hip/hip_runtime.h>
#include <hip/hip_bf16.h>
#include <stdint.h>

typedef unsigned int u32;
typedef unsigned short u16;
typedef unsigned char u8;
typedef u32 u32x4 __attribute__((ext_vector_type(4)));
typedef u32 u32x2 __attribute__((ext_vector_type(2)));
typedef short s16x2 __attribute__((ext_vector_type(2)));
typedef short s16x4 __attribute__((ext_vector_type(4)));
typedef __bf16 bf16x8 __attribute__((ext_vector_type(8)));
typedef __bf16 bf16x2 __attribute__((ext_vector_type(2)));
typedef float f32x4 __attribute__((ext_vector_type(4)));
#define LAS __attribute__((address_space(3)))

constexpr int NN = 50000;
constexpr int NBK = 256;
constexpr int NB1 = 250, CH = 3200;
constexpr int GPB = 13;
constexpr int SLOTS = 24;
constexpr int CAP2 = 192;

constexpr int DELAY = 50;
constexpr int NSH = 4;
constexpr int NREP = 64;

constexpr int L_LO = 0;
constexpr int L_NUL = L_LO + GPB * 2 * CAP2 * 4;
constexpr int L_DUMP = L_NUL + 32;
constexpr int L_OH = L_DUMP + 32;
constexpr int L_CNT = L_OH + L_OH / 2;
constexpr int L_TAB = L_CNT + 256;
constexpr int L_W = (L_TAB + GPB * 128 + 255) / 256 * 256;
constexpr int L_RING = L_W + 24576;
constexpr int RINGH = GPB * 4096;
constexpr int L_K2 = L_RING + 2 * RINGH;
constexpr int L_STAGE = L_RING;
constexpr int L_CNTA = L_STAGE + 32768;
static_assert(L_K2 <= 163840 && 32768 + 1040 <= RINGH, "lds");
static_assert(L_OH % 32 == 0 && L_CNT % 16 == 0 && L_TAB % 16 == 0, "align");
enum { F_BC = 56, F_ARR2 = 60, F_W = 61, F_GO2 = 62 };

constexpr size_t W_LINE = 0;
constexpr size_t W_OVF = W_LINE + (size_t)NBK * NB1 * 128;
constexpr size_t W_HDR = W_OVF + (size_t)NB1 * CH * 16;
constexpr size_t W_BAR = W_HDR + 2048;
constexpr size_t W_T1 = W_BAR + 512 * 256;
static_assert(W_OVF % 128 == 0 && W_BAR % 256 == 0 && W_T1 % 128 == 0 && NREP * NSH == 256, "align");
constexpr u32 HKEY = 0x9e3779b9u;
constexpr unsigned long long TMO = 20000000ull;

__device__ __forceinline__ u32 lds_addr(const void* p) { return (u32)(uintptr_t)(const LAS char*)p; }
__device__ __forceinline__ u32 bstart(u32 b) { return (b * 3125u) >> 4; }
__device__ __forceinline__ u32 bucket_of(u32 d) { return __umulhi(d * 16u + 15u, 1374390u); }

__device__ __forceinline__ u32x4 make_rsrc(const void* base, u32 bytes) {
    const uint64_t a = (uint64_t)base;
    u32x4 r;
    r[0] = (u32)a;
    r[1] = (u32)(a >> 32) & 0xffffu;
    r[2] = bytes;
    r[3] = 0x00020000u;
    return r;
}

template <bool SC1>
__device__ __forceinline__ void dma4(u32x4 rsrc, u32 lds, u32 o0, u32 o1, u32 o2, u32 o3) {
    u32 keep;
    if (SC1)
        asm volatile(
            "s_waitcnt lgkmcnt(0)\n\t"
            "s_mov_b32 %0, m0\n\t"
            "s_mov_b32 m0, %1\n\t"
            "s_nop 0\n\t"
            "buffer_load_dwordx4 %2, %6, 0 offen sc1 lds\n\t"
            "buffer_load_dwordx4 %3, %6, 0 offen offset:1024 sc1 lds\n\t"
            "buffer_load_dwordx4 %4, %6, 0 offen offset:2048 sc1 lds\n\t"
            "buffer_load_dwordx4 %5, %6, 0 offen offset:3072 sc1 lds\n\t"
            "s_mov_b32 m0, %0"
            : "=&s"(keep)
            : "s"(lds), "v"(o0), "v"(o1), "v"(o2), "v"(o3), "s"(rsrc)
            : "memory");
    else
        asm volatile(
            "s_waitcnt lgkmcnt(0)\n\t"
            "s_mov_b32 %0, m0\n\t"
            "s_mov_b32 m0, %1\n\t"
            "s_nop 0\n\t"
            "buffer_load_dwordx4 %2, %6, 0 offen lds\n\t"
            "buffer_load_dwordx4 %3, %6, 0 offen offset:1024 lds\n\t"
            "buffer_load_dwordx4 %4, %6, 0 offen offset:2048 lds\n\t"
            "buffer_load_dwordx4 %5, %6, 0 offen offset:3072 lds\n\t"
            "s_mov_b32 m0, %0"
            : "=&s"(keep)
            : "s"(lds), "v"(o0), "v"(o1), "v"(o2), "v"(o3), "s"(rsrc)
            : "memory");
}
__device__ __forceinline__ void dma2(u32x4 rsrc, u32 lds, u32 o0, u32 o1) {
    u32 keep;
    asm volatile(
        "s_waitcnt lgkmcnt(0)\n\t"
        "s_mov_b32 %0, m0\n\t"
        "s_mov_b32 m0, %1\n\t"
        "s_nop 0\n\t"
        "buffer_load_dwordx4 %2, %4, 0 offen lds\n\t"
        "buffer_load_dwordx4 %3, %4, 0 offen offset:1024 lds\n\t"
        "s_mov_b32 m0, %0"
        : "=&s"(keep)
        : "s"(lds), "v"(o0), "v"(o1), "s"(rsrc)
        : "memory");
}
#define WAIT_VM(n) asm volatile("s_waitcnt vmcnt(" #n ")" ::: "memory")
#define RLX_AGENT __ATOMIC_RELAXED, __HIP_MEMORY_SCOPE_AGENT
#define RLX_WG __ATOMIC_RELAXED, __HIP_MEMORY_SCOPE_WORKGROUP
__device__ __forceinline__ u32x4 ld16(const void* base, u32 bytes, u32 off) {
    return __builtin_amdgcn_raw_buffer_load_b128(__builtin_amdgcn_make_buffer_rsrc((void*)base, 0, bytes, 0x00020000), off, 0, 16);
}
__device__ __forceinline__ void st16(void* p, u32x4 v) {
    asm volatile("global_store_dwordx4 %0, %1, off sc1\n\ts_nop 1" ::"v"(p), "v"(v) : "memory");
}

__device__ __forceinline__ u32 pack_bf16(float a, float b) {
    bf16x2 v;
    v[0] = (__bf16)a;
    v[1] = (__bf16)b;
    return __builtin_bit_cast(u32, v);
}
__device__ __forceinline__ float bf_lo(u32 v) { return __uint_as_float(v << 16); }
__device__ __forceinline__ float bf_hi(u32 v) { return __uint_as_float(v & 0xffff0000u); }

struct Lane {
    u32 coff[4];
    u32 tra[4];
    u32 sh;
    int lane;
};
__device__ __forceinline__ Lane make_lane(int lane) {
    Lane L;
    L.lane = lane;
    const int a = lane >> 3, p = lane & 7;
#pragma unroll
    for (int i = 0; i < 4; ++i) L.coff[i] = (u32)(((p ^ ((((a >> 1) & 1) << 1) | ((i & 1) << 2))) << 4) + 4096 - 1024 * i);
    const int g = lane >> 4, q = (lane & 15) >> 2, pp = lane & 3;
    const int sw = ((g & 1) << 1) | ((q & 1) << 2);
#pragma unroll
    for (int c = 0; c < 4; ++c)
        L.tra[c] = (u32)(128 * (8 * (q & 1) + 2 * g + (q >> 1)) + 16 * ((2 * c + (pp >> 1)) ^ sw) + 8 * (pp & 1));
    L.sh = (u32)(15 - (lane & 15)) * 0x10001u;
    return L;
}
__device__ __forceinline__ bf16x8 afrag(const char* buf, const Lane& L, int c) {
    const s16x4 a0 = __builtin_amdgcn_ds_read_tr16_b64_v4i16((LAS s16x4*)(buf + L.tra[c]));
    const s16x4 a1 = __builtin_amdgcn_ds_read_tr16_b64_v4i16((LAS s16x4*)(buf + L.tra[c] + 2048));
    return __builtin_bit_cast(bf16x8, __builtin_shufflevector(a0, a1, 0, 1, 2, 3, 4, 5, 6, 7));
}
__device__ __forceinline__ u32 row_off(u32 lo, u32 add) {
    u32 r;
    asm("v_mad_u32_u16 %0, %1, %2, %3" : "=v"(r) : "v"(lo), "s"(128u), "v"(add));
    return r;
}
__device__ __forceinline__ u32 sel2(u32 oh, u32 lo0, u32 lo1, u32 sh) {
    const s16x2 m = (__builtin_bit_cast(s16x2, oh) << __builtin_bit_cast(s16x2, sh)) >> 15;
    return __builtin_amdgcn_perm(lo1, lo0, 0x07060302u) & __builtin_bit_cast(u32, m);
}
__device__ __forceinline__ int make_tab(char* smem, int wv, int lane) {
    const u32x4 cv = *(const u32x4*)(smem + L_CNT + 16 + wv * 16);
    const u32 c0 = __builtin_amdgcn_readfirstlane(cv[0]), c1 = __builtin_amdgcn_readfirstlane(cv[1]);
    const u32 c2 = __builtin_amdgcn_readfirstlane(cv[2]), c3 = __builtin_amdgcn_readfirstlane(cv[3]);
    const u32 b0 = (u32)wv * 48u;
    const u32 n0 = (c0 + 31u) >> 5, s1 = max((c1 + 4u) >> 5, n0);
    const u32 e2 = (c2 + 31u) >> 5, s3 = max((c3 + 4u) >> 5, e2);
    const u32 ea = n0 - b0, eb = ea + (e2 - s1), nb = eb + (b0 + 48u - s3), v = (u32)lane;
    u32 pb = v < eb ? s1 - ea : s3 - eb;
    pb = v < ea ? b0 : pb;
    const u32 a = v < nb ? (pb + v) * 32u : (u32)L_NUL;
    *(u16*)(smem + L_TAB + wv * 128 + lane * 2) = (u16)a;
    __builtin_amdgcn_wave_barrier();
    return (int)((nb + 3u) >> 2);
}
struct Blk { u32x4 ix, l0, l1, oh; };
__device__ __forceinline__ Blk read_blk(const char* smem, u32 a, const Lane& L) {
    Blk B;
    B.ix = *(const u32x4*)(smem + a + (L.lane & 8) * 2);
    B.l0 = *(const u32x4*)(smem + a);
    B.l1 = *(const u32x4*)(smem + a + 16);
    B.oh = *(const u32x4*)(smem + L_OH + (a >> 1));
    return B;
}
template <bool SC1>
__device__ __forceinline__ void issue_blk(u32x4 rsrc, const Blk& B, u32 buf, const Lane& L) {
    dma4<SC1>(rsrc, buf, row_off(B.ix[0], L.coff[0]), row_off(B.ix[1], L.coff[1]), row_off(B.ix[2], L.coff[2]), row_off(B.ix[3], L.coff[3]));
}
__device__ __forceinline__ u32x4 sel_blk(const Blk& B, const Lane& L) {
    u32x4 b;
    b[0] = sel2(B.oh[0], B.l0[0], B.l0[1], L.sh);
    b[1] = sel2(B.oh[2], B.l1[0], B.l1[1], L.sh);
    b[2] = sel2(B.oh[1], B.l0[2], B.l0[3], L.sh);
    b[3] = sel2(B.oh[3], B.l1[2], B.l1[3], L.sh);
    return b;
}
__device__ __forceinline__ void own_rows(u32x4 rsrc, u32 buf, int node0, int lane) {
    const u32 o = (u32)(node0 + (lane >> 3)) * 128u + (u32)(((lane & 7) ^ (lane >> 4)) << 4) + 4096u;
    dma2(rsrc, buf, o, o ^ 64u);
}
struct Prime { u32x4 off[2], bq[2]; u32 an; };
__device__ __forceinline__ Prime make_prime(const char* smem, int wv, const Lane& L) {
    const u16* tp = (const u16*)(smem + L_TAB + wv * 128) + (L.lane >> 4);
    Prime P;
#pragma unroll
    for (int h = 0; h < 2; ++h) {
        const Blk B = read_blk(smem, tp[4 * h], L);
#pragma unroll
        for (int i = 0; i < 4; ++i) P.off[h][i] = row_off(B.ix[i], L.coff[i]);
        P.bq[h] = sel_blk(B, L);
    }
    P.an = tp[8];
    return P;
}
template <bool SC1, bool OWN>
__device__ __forceinline__ void gather_group(f32x4 (&acc)[4], u32x4 rsrc, char* smem, int wv, int ns, const Lane& L, const Prime& P,
                                             int node0) {
    char* ring = smem + L_RING + wv * 4096;
    const u32 rb = lds_addr(ring);
    const u16* tp = (const u16*)(smem + L_TAB + wv * 128) + (L.lane >> 4);
    u32x4 bq[2] = {P.bq[0], P.bq[1]};
    __builtin_amdgcn_s_setprio(3);
#pragma unroll
    for (int h = 0; h < 2; ++h)
        if (h < ns) dma4<SC1>(rsrc, rb + h * RINGH, P.off[h][0], P.off[h][1], P.off[h][2], P.off[h][3]);
        else if (OWN && h == ns) own_rows(rsrc, rb + h * RINGH, node0, L.lane);
    u32 an = P.an;
    int rem = ns;
    while (rem > 0) {
#pragma unroll
        for (int h = 0; h < 2; ++h) {
            if (h == 1 && rem == 0) break;
            if (rem == 7) __builtin_amdgcn_s_setprio(2);
            else if (rem == 5) __builtin_amdgcn_s_setprio(1);
            else if (rem == 3) __builtin_amdgcn_s_setprio(0);
            const Blk B = read_blk(smem, an, L);
            an = tp[4 * (h + 3)];
            if (rem > 1) WAIT_VM(4); else if (OWN) WAIT_VM(2); else WAIT_VM(0);
            bf16x8 a[4];
#pragma unroll
            for (int c = 0; c < 4; ++c) a[c] = afrag(ring + h * RINGH, L, c);
            if (rem > 2) issue_blk<SC1>(rsrc, B, rb + h * RINGH, L);
            else if (OWN && rem == 2) own_rows(rsrc, rb + h * RINGH, node0, L.lane);
#pragma unroll
            for (int c = 0; c < 4; ++c)
                acc[c] = __builtin_amdgcn_mfma_f32_16x16x32_bf16(a[c], __builtin_bit_cast(bf16x8, bq[h]), acc[c], 0, 0, 0);
            bq[h] = sel_blk(B, L);
            --rem;
        }
        tp += 8;
    }
    __builtin_amdgcn_s_setprio(0);
}

__device__ __forceinline__ u32 class4(u32 lo) { return ((lo >> 5) & 8u) | (((lo + 7768u) >> 13) & 4u); }
__device__ __forceinline__ void put_record(char* smem, u32 lo, u32 meta) {
    const u32 q = class4(lo);
    u32 a = atomicAdd((u32*)(smem + L_CNT + (meta & 0xf0u) + q), (q & 4u) ? (u32)-4 : 4u);
    a = min(a, (u32)L_DUMP);
    *(u32*)(smem + a) = lo;
    *(u16*)(smem + L_OH + (a >> 1)) = (u16)(1u << (meta & 15u));
}
struct Pos { u32 a[3], oh[3]; bool ok[3]; };
__device__ __forceinline__ Pos claim_chunk(char* smem, u32x4 v) {
    Pos P;
#pragma unroll
    for (int k = 0; k < 3; ++k) {
        const u32 g = (v[3] >> (8 * k)) & 0xf0u;
        const u32 m = class4(v[k]);
        u32 dir;
        asm("v_bfe_i32 %0, %1, 2, 1\n\tv_lshl_or_b32 %0, %0, 3, 4" : "=v"(dir) : "v"(m));
        if (k == 0) asm("v_lshlrev_b16_sdwa %0, %1, %2 dst_sel:DWORD dst_unused:UNUSED_PAD src0_sel:BYTE_0 src1_sel:DWORD" : "=v"(P.oh[k]) : "v"(v[3]), "v"(1u));
        if (k == 1) asm("v_lshlrev_b16_sdwa %0, %1, %2 dst_sel:DWORD dst_unused:UNUSED_PAD src0_sel:BYTE_1 src1_sel:DWORD" : "=v"(P.oh[k]) : "v"(v[3]), "v"(1u));
        if (k == 2) asm("v_lshlrev_b16_sdwa %0, %1, %2 dst_sel:DWORD dst_unused:UNUSED_PAD src0_sel:BYTE_2 src1_sel:DWORD" : "=v"(P.oh[k]) : "v"(v[3]), "v"(1u));
        P.ok[k] = g != 0u;
        P.a[k] = dir;
        if (P.ok[k]) P.a[k] = atomicAdd((u32*)(smem + L_CNT + (g | m)), dir);
    }
    return P;
}
__device__ __forceinline__ void write_chunk(char* smem, Pos& P, u32x4 v) {
    asm volatile("" : "+v"(P.a[0]), "+v"(P.a[1]), "+v"(P.a[2]));
#pragma unroll
    for (int k = 0; k < 3; ++k) {
        const u32 p = min(P.a[k], (u32)L_DUMP);
        if (P.ok[k]) {
            *(u32*)(smem + p) = v[k];
            *(u16*)(smem + L_OH + (p >> 1)) = (u16)P.oh[k];
        }
    }
}
__device__ __forceinline__ void put_surplus(char* smem, u32x4 r, u32 bucket) {
    if (bucket_of(r[1]) == bucket) put_record(smem, r[0], r[1] - bstart(bucket) + 16u);
}
__device__ __forceinline__ u32 wait_hdr(const unsigned long long* hdr, int b, u32 epoch) {
    const unsigned long long t0 = __builtin_amdgcn_s_memrealtime();
    for (;;) {
        const unsigned long long h = __hip_atomic_load(hdr + b, RLX_AGENT);
        if (__builtin_amdgcn_readfirstlane((u32)(h >> 32)) == (epoch ^ HKEY)) return min((u32)__builtin_amdgcn_readfirstlane((u32)h), (u32)CH);
        if (__builtin_amdgcn_s_memrealtime() - t0 > TMO) return 0u;
        __builtin_amdgcn_s_sleep(2);
    }
}
template <bool FIRST>
__device__ __forceinline__ void init_lists(char* smem, int t) {
#pragma unroll
    for (int h = 0; h < 2; ++h) {
        const int i = t + h * 1024;
        if (i < L_CNT / 16) *(u32x4*)(smem + i * 16) = (u32x4)(i < L_OH / 16 ? 0xffffffffu : 0u);
    }
    if (t >= 960) {
        const u32 b = (u32)t - 960u - 4u;
        if (b < 4u * GPB) *(u32*)(smem + L_CNT + 16 + b * 4) = (b >> 1) * (CAP2 * 4u) + ((b & 1u) ? (CAP2 - 1) * 4u : 0u);
        else if (FIRST) *(u32*)(smem + L_CNT + 16 + (int)b * 4) = 0u;
    }
}

template <bool SC1>
__device__ __forceinline__ void slow_bucket(char* smem, f32x4 (&acc)[4], const char* bl, const char* ovf,
                                            const unsigned long long* hdr, u32 epoch, u32 bucket,
                                            u32x4 rsrc, int t, int wv, const Lane& L) {
    int blk = -((NB1 + 7) / 8);
    u32 o0 = 0, n = 0;
    for (;;) {
        if (blk >= 0 && o0 >= n) {
            u32* bc = (u32*)(smem + L_CNT) + F_BC;
            __syncthreads();
            if (wv == 0) {
                for (; blk < NB1; ++blk)
                    if (__any(__hip_atomic_load((const u32*)(bl + blk * 128 + (t & 7) * 16 + 12), RLX_AGENT) >> 31)) break;
                n = blk < NB1 ? wait_hdr(hdr, blk, epoch) : 0u;
                if (t == 0) { bc[0] = (u32)blk; bc[1] = n; }
            }
            __syncthreads();
            blk = __builtin_amdgcn_readfirstlane((int)bc[0]);
            n = __builtin_amdgcn_readfirstlane(bc[1]);
            if (blk >= NB1) break;
            o0 = 0;
        }
        __syncthreads();
        init_lists<false>(smem, t);
        __syncthreads();
        if (blk < 0) {
            const int c = (blk + (NB1 + 7) / 8) * 64 + t;
            if (t < 64 && c < NB1 * 8) { const u32x4 v = ld16(bl, NB1 * 128, c * 16); Pos P = claim_chunk(smem, v); write_chunk(smem, P, v); }
            ++blk;
        } else {
            if (t < CAP2 && o0 + t < n) put_surplus(smem, ld16(ovf, NB1 * CH * 16, (blk * CH + o0 + t) * 16), bucket);
            o0 += CAP2;
            if (o0 >= n) ++blk;
        }
        __syncthreads();
        if (wv < GPB) {
            const int ns = make_tab(smem, wv, L.lane);
            gather_group<SC1, false>(acc, rsrc, smem, wv, ns, L, make_prime(smem, wv, L), 0);
        }
    }
}

__device__ __forceinline__ void store_tile(char* __restrict__ outp, int node0, int nrows, const u32x2 (&v)[4], char* buf, int lane) {
    const int n = lane & 15, q = lane >> 4;
#pragma unroll
    for (int c = 0; c < 4; ++c) *(u32x2*)(buf + n * 128 + (((2 * c + (q >> 1)) ^ (n & 7)) << 4) + 8 * (q & 1)) = v[c];
    __builtin_amdgcn_wave_barrier();
    char* p = outp + (size_t)node0 * 128 + lane * 16;
    const int r = lane >> 3;
#pragma unroll
    for (int h = 0; h < 2; ++h)
        if (r + 8 * h < nrows) st16(p + h * 1024, *(const u32x4*)(buf + h * 1024 + r * 128 + (((lane & 7) ^ r) << 4)));
}
__device__ __forceinline__ void dense(f32x4 (&o)[4], const char* smem, int k, const u32x2 (&f)[4], const Lane& L) {
#pragma unroll
    for (int kk = 0; kk < 2; ++kk) {
        u32x4 b;
        b[0] = f[2 * kk][0]; b[1] = f[2 * kk][1]; b[2] = f[2 * kk + 1][0]; b[3] = f[2 * kk + 1][1];
#pragma unroll
        for (int c = 0; c < 4; ++c)
            o[c] = __builtin_amdgcn_mfma_f32_16x16x32_bf16(afrag(smem + L_W + (2 * k + kk) * 4096, L, c), __builtin_bit_cast(bf16x8, b), o[c], 0, 0, 0);
    }
}

__device__ __forceinline__ void bar_arrive(u32* bar, int lane) {
    if (lane < NREP) __hip_atomic_fetch_add(bar + (lane * NSH + blockIdx.x % NSH) * 64, 1u, RLX_AGENT);
}
__device__ __forceinline__ void bar_wait(const u32* bar, int lane) {
    const u32* pb = bar + ((blockIdx.x / NSH) % NREP * NSH + (lane % NSH)) * 64;
    const unsigned long long t0 = __builtin_amdgcn_s_memrealtime();
    u32 c = lane < NSH ? 0u : (u32)(NBK / NSH);
    while (__builtin_amdgcn_s_memrealtime() - t0 < TMO) {
        if (c != (u32)(NBK / NSH)) c = __hip_atomic_load(pb, RLX_AGENT);
        if (__all(c == (u32)(NBK / NSH))) break;
        __builtin_amdgcn_s_sleep(2);
    }
}
__device__ __forceinline__ void lds_wait(const u32* f, u32 n) {
    while (__hip_atomic_load(f, RLX_WG) < n) __builtin_amdgcn_s_sleep(1);
    asm volatile("" ::: "memory");
}

__device__ __forceinline__ void wait_lines(u32x4& v, const char* bl, u32 off, u32 tag, int lane) {
    const bool in = off < (u32)(NB1 * 128);
    unsigned long long bad = __ballot(in && ((v[3] >> 24) & 0x7fu) != tag);
    if (bad != 0ull) {
        const unsigned long long t0 = __builtin_amdgcn_s_memrealtime();
        do {
            __builtin_amdgcn_s_sleep(2);
            asm volatile("" ::: "memory");
            if ((bad >> (lane & 56)) & 0xffull) v = ld16(bl, NB1 * 128, off);
            asm volatile("" : "+v"(v));
            bad = __ballot(in && ((v[3] >> 24) & 0x7fu) != tag);
        } while (bad != 0ull && __builtin_amdgcn_s_memrealtime() - t0 < TMO);
        if (bad != 0ull) v[3] = 0u;
        __builtin_amdgcn_s_waitcnt(0xC07F);
    }
}
__device__ __forceinline__ u32x4 first_surplus(unsigned long long f, int b0, const char* ovf, int lane) {
    return ld16(ovf, NB1 * CH * 16, (u32)((b0 + (__builtin_ctzll(f) >> 3)) * CH + lane) * 16u);
}
__device__ __forceinline__ void take_surplus(char* smem, unsigned long long f, int b0, u32x4 r, const char* ovf,
                                             const unsigned long long* hdr, u32 epoch, u32 bucket, int lane) {
    for (;;) {
        const int j = __builtin_ctzll(f) >> 3, b = b0 + j;
        const u32 m = (u32)(f >> (8 * j)) & 0xffu;
        bool done = false;
        for (int it = 0; m < 64u; ++it) {
            const bool mine = r[2] == epoch && r[3] == ~epoch && bucket_of(r[1] & 0xffffu) == bucket;
            if ((u32)__builtin_popcountll(__ballot(mine)) == m) {
                if (mine) put_surplus(smem, r, bucket);
                done = true;
                break;
            }
            if (it == 3) break;
            asm volatile("" ::: "memory");
            r = first_surplus(f, b0, ovf, lane);
        }
        if (!done) {
            const u32 n = wait_hdr(hdr, b, epoch);
            for (u32 o = lane; o < n; o += 64) put_surplus(smem, ld16(ovf, NB1 * CH * 16, (b * CH + o) * 16u), bucket);
        }
        f &= ~(0xffull << (8 * j));
        if (f == 0ull) break;
        r = first_surplus(f, b0, ovf, lane);
    }
}

__global__ __launch_bounds__(1024, 4) void k_cheb(const u32* __restrict__ src, const u32* __restrict__ dst,
                                               const u16* __restrict__ ewp, const char* __restrict__ x,
                                               const char* __restrict__ w, char* ws, char* __restrict__ out) {
    __shared__ __attribute__((aligned(256))) char smem[L_K2];
    char* lines = ws + W_LINE;
    char* ovf = ws + W_OVF;
    unsigned long long* hdr = (unsigned long long*)(ws + W_HDR);
    u32* bar = (u32*)(ws + W_BAR);
    char* t1 = ws + W_T1;
    const int t = threadIdx.x, lane = t & 63, wv = __builtin_amdgcn_readfirstlane(t >> 6);
    const int blk = blockIdx.x;
    const int bucket = (blk & 7) * 32 + (blk >> 3);
    const bool active = wv < GPB;
    const int node0 = (int)bstart(bucket) + wv * 16;
    const int nrows = min((int)bstart(bucket + 1) - node0, 16);
    char* ring = smem + L_RING + wv * 4096;
    u32* lflag = (u32*)(smem + L_CNT);
    Lane L = make_lane(lane);
    u32 epoch;
    {
        u32* stage = (u32*)(smem + L_STAGE);
        u32* cnt = (u32*)(smem + L_CNTA);
        const u32 nb = blk < NB1 ? (u32)CH : 0u;
        const size_t e0i = (size_t)min(blk, NB1 - 1) * CH;
        const u32x4 d = __builtin_amdgcn_raw_buffer_load_b128(__builtin_amdgcn_make_buffer_rsrc((void*)(dst + e0i), 0, nb * 4u, 0x00020000), t * 16, 0, 2);
        const u32x4 s = __builtin_amdgcn_raw_buffer_load_b128(__builtin_amdgcn_make_buffer_rsrc((void*)(src + e0i), 0, nb * 4u, 0x00020000), t * 16, 0, 2);
        const u32x2 ew = __builtin_amdgcn_raw_buffer_load_b64(__builtin_amdgcn_make_buffer_rsrc((void*)(ewp + e0i), 0, nb * 2u, 0x00020000), t * 8, 0, 2);
        const u32 e1 = __hip_atomic_load(bar + 288 * 64, RLX_AGENT) + 1u;
        if (blk == NBK - 1 && t < 256) __hip_atomic_store(bar + t * 64, 0u, RLX_AGENT);
        if (t < 512) *(u32x4*)&stage[3 * 2048 + t * 4] = (u32x4)0u;
        else if (t < 512 + 65) *(u32x4*)&cnt[(t - 512) * 4] = (u32x4)0u;
        init_lists<true>(smem, t);
        asm volatile("" : "+v"(L.coff[0]), "+v"(L.coff[1]), "+v"(L.coff[2]), "+v"(L.coff[3]), "+v"(L.tra[0]), "+v"(L.tra[1]),
                     "+v"(L.tra[2]), "+v"(L.tra[3]), "+v"(L.sh));
        __syncthreads();
        __builtin_amdgcn_sched_barrier(0);
        const bool bin = t < CH / 4 && blk < NB1;
        u32 rk[4], bk[4];
#pragma unroll
        for (int i = 0; i < 4; ++i) {
            bk[i] = bucket_of((u32)d[i]);
            rk[i] = 0u;
            if (bin) rk[i] = atomicAdd(&cnt[bk[i]], 1u);
        }
        asm volatile("" :: "v"(s), "v"(ew));
        if (bin) {
#pragma unroll
            for (int i = 0; i < 4; ++i) {
                const u32 lo = (u32)s[i] | ((i & 1) ? (ew[i >> 1] & 0xffff0000u) : (ew[i >> 1] << 16));
                const u32 c = bk[i] * 8u + (rk[i] & 7u), sub = rk[i] >> 3;
                if (__builtin_expect(rk[i] < (u32)SLOTS, 1)) {
                    stage[sub * 2048u + c] = lo;
                    ((u8*)&stage[3 * 2048 + c])[sub] = (u8)((u32)d[i] - bstart(bk[i]) + 16u);
                } else {
                    const u32 o = atomicAdd(&cnt[256], 1u);
                    u32x4 r;
                    r[0] = lo; r[1] = (u32)d[i]; r[2] = e1; r[3] = ~e1;
                    st16(ovf + (size_t)(blk * CH + o) * 16, r);
                }
            }
        }
        epoch = __builtin_amdgcn_readfirstlane(e1);
        if (blk == NBK - 1) WAIT_VM(0);
        __syncthreads();
        if (blk < NB1) {
#pragma unroll
            for (int h = 0; h < 2; ++h) {
                const int c = t + h * 1024;
                const u32 m = min(max(cnt[c >> 3], (u32)SLOTS) - (u32)SLOTS, 255u);
                u32x4 v;
                v[0] = stage[c]; v[1] = stage[2048 + c]; v[2] = stage[4096 + c];
                v[3] = stage[6144 + c] | ((epoch & 0x7fu) << 24) | ((m >> (c & 7)) << 31);
                st16(lines + ((size_t)(c >> 3) * NB1 + blk) * 128 + (c & 7) * 16, v);
            }
        } else if (blk == NBK - 1 && t < 32) {
            __hip_atomic_store((unsigned long long*)(bar + (256 + t) * 64), (unsigned long long)epoch | ((unsigned long long)(epoch ^ HKEY) << 32), RLX_AGENT);
        }
    }
    const char* bl = lines + (size_t)bucket * (NB1 * 128);
    {
        __builtin_amdgcn_s_sleep(DELAY);
        if (blk >= NB1) __builtin_amdgcn_s_sleep(30);
        u32x4 v0 = ld16(bl, NB1 * 128, t * 16);
        u32x4 v1 = ld16(bl, NB1 * 128, (t + 1024) * 16);
        u32x4 r0 = 0u, r1 = 0u;
        wait_lines(v0, bl, t * 16, epoch & 0x7fu, lane);
        const unsigned long long f0 = __ballot(v0[3] >> 31);
        if (__builtin_expect(f0 != 0ull, 0)) r0 = first_surplus(f0, wv * 8, ovf, lane);
        {
            Pos P0 = claim_chunk(smem, v0);
            write_chunk(smem, P0, v0);
        }
        __builtin_amdgcn_sched_barrier(0);
        wait_lines(v1, bl, (t + 1024) * 16, epoch & 0x7fu, lane);
        const unsigned long long f1 = __ballot(v1[3] >> 31);
        if (__builtin_expect(f1 != 0ull, 0)) r1 = first_surplus(f1, wv * 8 + 128, ovf, lane);
        {
            Pos P1 = claim_chunk(smem, v1);
            write_chunk(smem, P1, v1);
        }
        {
            u32* cnt = (u32*)(smem + L_CNTA);
            const u32 novf = cnt[256];
            if (__builtin_expect(novf != 0u, 0)) {
                WAIT_VM(0);
                u32 old = 0;
                if (lane == 0) old = atomicAdd(&cnt[257], 1u);
                if (old == 15u) __hip_atomic_store(hdr + blk, (unsigned long long)novf | ((unsigned long long)(epoch ^ HKEY) << 32), RLX_AGENT);
            }
        }
        if (__builtin_expect(f0 != 0ull, 0)) take_surplus(smem, f0, wv * 8, r0, ovf, hdr, epoch, bucket, lane);
        if (__builtin_expect(f1 != 0ull, 0)) take_surplus(smem, f1, wv * 8 + 128, r1, ovf, hdr, epoch, bucket, lane);
    }
    __syncthreads();
    if (!active) {
        __builtin_amdgcn_s_setprio(3);
        const u32x4 wrs = make_rsrc(w - 4096, 3u * 64u * 128u + 4096u);
        const int a = lane >> 3;
#pragma unroll
        for (int h = 0; h < 2; ++h) {
            const int ks = (wv - GPB) * 2 + h;
            const u32 rb = (u32)(32 * ks + 4 * (a >> 1) + 2 * (a & 1)) * 128u;
            dma4<false>(wrs, lds_addr(smem + L_W + ks * 4096), rb + L.coff[0], rb + 128u + L.coff[1], rb + 2048u + L.coff[2], rb + 2176u + L.coff[3]);
        }
    }
    bool slow;
    {
        const u32x2 c = *(const u32x2*)(smem + L_CNT + 16 + min(lane, 2 * GPB - 1) * 8);
        slow = __builtin_expect(__any((int)c[0] > (int)c[1] + 4), 0);
    }
    if (!active) WAIT_VM(0);
    if (wv == 15) {
        const unsigned long long* rp = (const unsigned long long*)(bar + (256 + (blk & 31)) * 64);
        const unsigned long long t0 = __builtin_amdgcn_s_memrealtime();
        while (__hip_atomic_load(rp, RLX_AGENT) != ((unsigned long long)epoch | ((unsigned long long)(epoch ^ HKEY) << 32)) &&
               __builtin_amdgcn_s_memrealtime() - t0 < TMO) __builtin_amdgcn_s_sleep(4);
    }
    if (!active && lane == 0) __hip_atomic_fetch_add(lflag + F_W, 1u, RLX_WG);
    if (!slow && !active) return;
    f32x4 acc[4], o[4];
#pragma unroll
    for (int c = 0; c < 4; ++c) { acc[c] = 0.f; o[c] = 0.f; }
    int ns = 0;
    {
        const u32x4 rsrc = make_rsrc(x - 4096, (u32)NN * 128u + 4096u);
        if (__builtin_expect(!slow, 1)) {
            ns = make_tab(smem, wv, lane);
            gather_group<false, true>(acc, rsrc, smem, wv, ns, L, make_prime(smem, wv, L), node0);
        } else {
            slow_bucket<false>(smem, acc, bl, ovf, hdr, epoch, bucket, rsrc, t, wv, L);
            if (active) own_rows(rsrc, lds_addr(ring), node0, lane);
        }
    }
    Prime P2;
    u32x2 xf[4];
    if (active) {
        u32x2 tf[4];
#pragma unroll
        for (int c = 0; c < 4; ++c) {
            tf[c][0] = pack_bf16(acc[c][0], acc[c][1]);
            tf[c][1] = pack_bf16(acc[c][2], acc[c][3]);
            acc[c] = 0.f;
        }
        store_tile(t1, node0, nrows, tf, ring + (~ns & 1) * RINGH, lane);
        lds_wait(lflag + F_W, 3u);
        dense(o, smem, 1, tf, L);
        WAIT_VM(0);
        u32 old = 0;
        if (lane == 0) old = __hip_atomic_fetch_add(lflag + F_ARR2, 1u, RLX_WG);
        const bool last = __builtin_amdgcn_readfirstlane(old) == (u32)(GPB - 1);
        if (last) bar_arrive(bar, lane);
        __builtin_amdgcn_sched_barrier(0);
        const int n = lane & 15, q = lane >> 4;
#pragma unroll
        for (int c = 0; c < 4; ++c)
            xf[c] = *(const u32x2*)(ring + (ns & 1) * RINGH + n * 128 + (((2 * c + (q >> 1)) ^ (n >> 1)) << 4) + 8 * (q & 1));
        dense(o, smem, 0, xf, L);
        P2 = make_prime(smem, wv, L);
        asm volatile("" : "+v"(P2.off[0]), "+v"(P2.off[1]), "+v"(P2.bq[0]), "+v"(P2.bq[1]), "+v"(P2.an));
        if (last) {
            bar_wait(bar, lane);
            if (lane == 0) __hip_atomic_store(lflag + F_GO2, 1u, RLX_WG);
            if (blk == NBK - 1 && lane == 0) __hip_atomic_store(bar + 288 * 64, epoch, RLX_AGENT);
        }
    }
    lds_wait(lflag + F_GO2, 1u);
    {
        const u32x4 rsrc = make_rsrc(t1 - 4096, (u32)NN * 128u + 4096u);
        if (__builtin_expect(!slow, 1)) gather_group<true, false>(acc, rsrc, smem, wv, ns, L, P2, 0);
        else slow_bucket<true>(smem, acc, bl, ovf, hdr, epoch, bucket, rsrc, t, wv, L);
    }
    if (!active) return;
    u32x2 f[4];
#pragma unroll
    for (int c = 0; c < 4; ++c) {
        f[c][0] = pack_bf16(2.f * acc[c][0] - bf_lo(xf[c][0]), 2.f * acc[c][1] - bf_hi(xf[c][0]));
        f[c][1] = pack_bf16(2.f * acc[c][2] - bf_lo(xf[c][1]), 2.f * acc[c][3] - bf_hi(xf[c][1]));
    }
    dense(o, smem, 2, f, L);
#pragma unroll
    for (int c = 0; c < 4; ++c) {
        f[c][0] = pack_bf16(o[c][0], o[c][1]);
        f[c][1] = pack_bf16(o[c][2], o[c][3]);
    }
    store_tile(out, node0, nrows, f, ring, lane);
}

extern "C" void kernel_launch(void* const* d_in, const int* in_sizes, int n_in, void* d_out, int out_size, void* d_ws,
                              size_t ws_size, hipStream_t stream) {
    k_cheb<<<NBK, 1024, 0, stream>>>((const u32*)d_in[1], (const u32*)d_in[2], (const u16*)d_in[3],
                                     (const char*)d_in[0], (const char*)d_in[4], (char*)d_ws, (char*)d_out);
}
```
